```python
import torch

def _softmax(x, axis=-1):
    return torch.softmax(x, dim=axis)
def _gelu(x):
    return torch.nn.functional.gelu(x, approximate="tanh")
def _silu(x):
    return torch.nn.functional.silu(x)
def _sigmoid(x):
    return torch.sigmoid(x)
def _layernorm(x, axis=-1, eps=1e-5):
    m = x.mean(dim=axis, keepdim=True)
    v = x.var(dim=axis, keepdim=True, unbiased=False)
    return (x - m) / torch.sqrt(v + eps)
def _scatter_add(tgt, idx, src, axis=0):
    return tgt.clone().index_add_(axis, idx, src)
def _segment_sum(src, seg, n):
    out = torch.zeros((n,) + src.shape[1:], dtype=src.dtype)
    return out.index_add_(0, seg, src)
def _pad(x, pw):
    flat = tuple(v for pair in reversed(pw) for v in pair)
    return torch.nn.functional.pad(x, flat)

kernel_name = 'standalone__gqa_block_cached__near_ridge'

def setup_inputs():

    _w = (lambda a: (lambda t: t.to(torch.bfloat16) if t.is_floating_point() else t)(torch.as_tensor(a)))
    B, Sq, D = 8, 64, 576
    Hq, Hkv, Sk, Dh = 32, 16, 2048, 128
    assert Hq % Hkv == 0, (Hq, Hkv)
    return {
        "x": _w((lambda *s: torch.randn(*s, dtype=torch.bfloat16) * 0.8)(B, Sq, D)),
        "k_cache": _w((lambda *s: torch.randn(*s, dtype=torch.bfloat16) * 0.8)(B, Hkv, Sk, Dh)),
        "v_cache": _w((lambda *s: torch.randn(*s, dtype=torch.bfloat16) * 0.05)(B, Hkv, Sk, Dh)),
        "wq": _w((lambda *s: torch.randn(*s, dtype=torch.bfloat16) * 0.8)(D, Hq * Dh) * (D ** -0.5)),
        "wo": _w((lambda *s: torch.randn(*s, dtype=torch.bfloat16) * 0.05)(Hq * Dh, D) * ((Hq * Dh) ** -0.5)),
    }

def reference(x, k_cache, v_cache, wq, wo):
    B, Sq, D = x.shape
    Hq, Dh = 32, 128
    Hkv = k_cache.shape[1]
    rep = Hq // Hkv
    q = (lambda x, *d: x.permute(*d))((x @ wq).reshape(B, Sq, Hq, Dh), 0, 2, 1, 3)
    k = (lambda x, n, axis: torch.repeat_interleave(x, n, dim=axis))(k_cache, rep, axis=1)
    v = (lambda x, n, axis: torch.repeat_interleave(x, n, dim=axis))(v_cache, rep, axis=1)
    scores = torch.einsum("bhqd,bhkd->bhqk", q, k) * (Dh ** -0.5)
    p = _softmax(scores, axis=-1)
    o = torch.einsum("bhqk,bhkd->bhqd", p, v)
    return (lambda x, *d: x.permute(*d))(o, 0, 2, 1, 3).reshape(B, Sq, Hq * Dh) @ wo
```

```cpp
#include <hip/hip_runtime.h>
#include <stdint.h>
#include <type_traits>

#define KV_NT 1
#define W_NT 0
#define LOOKA 6

#define DI __device__ __forceinline__
#define LAS __attribute__((address_space(3)))
#define GAS __attribute__((address_space(1)))
typedef __bf16 bf16x8 __attribute__((ext_vector_type(8)));
typedef __bf16 bf16x2 __attribute__((ext_vector_type(2)));
typedef short s16x4 __attribute__((ext_vector_type(4)));
typedef short s16x8 __attribute__((ext_vector_type(8)));
typedef float f32x2 __attribute__((ext_vector_type(2)));
typedef float f32x16 __attribute__((ext_vector_type(16)));
typedef unsigned u32x4 __attribute__((ext_vector_type(4)));
typedef unsigned u32x2 __attribute__((ext_vector_type(2)));
typedef unsigned long long u64;

constexpr int SQ = 64, DM = 576, HKV = 16, SK = 2048;
constexpr unsigned TILEB = 16384;
constexpr unsigned KR = 0, VR = 5 * TILEB;
constexpr unsigned SCR = 8 * TILEB;
constexpr unsigned LDS_BYTES = 10 * TILEB;
constexpr float QSCALE = 0.08838834764831845f * 1.4426950408889634f;
constexpr float THR = 8.f;

#define SBAR() __builtin_amdgcn_sched_barrier(0)
#define MFMA(a, b, c) __builtin_amdgcn_mfma_f32_32x32x16_bf16((a), (b), (c), 0, 0, 0)
#define VMCNT(n) asm volatile("s_waitcnt vmcnt(%0)" :: "i"(n) : "memory")
#define BARRIER() asm volatile("s_waitcnt lgkmcnt(0)\n\ts_barrier" ::: "memory")
#define RLX_AGENT __ATOMIC_RELAXED, __HIP_MEMORY_SCOPE_AGENT

DI unsigned cvtpk(float lo, float hi) { f32x2 v = {lo, hi}; bf16x2 b = __builtin_convertvector(v, bf16x2); return __builtin_bit_cast(unsigned, b); }
DI float bflo(unsigned u) { return __uint_as_float(u << 16); }
DI float bfhi(unsigned u) { return __uint_as_float(u & 0xffff0000u); }
DI bf16x8 lds_b128(unsigned a) { return *(const LAS bf16x8*)(uintptr_t)a; }
DI s16x4 lds_tr(unsigned a) { return __builtin_amdgcn_ds_read_tr16_b64_v4i16((LAS s16x4*)(uintptr_t)a); }
DI bf16x8 cat(s16x4 lo, s16x4 hi) { s16x8 r = __builtin_shufflevector(lo, hi, 0, 1, 2, 3, 4, 5, 6, 7); return __builtin_bit_cast(bf16x8, r); }
DI void lds_w128(unsigned a, u32x4 v) { *(LAS u32x4*)(uintptr_t)a = v; }
template <int NT> DI void dma_(const void* sbase, unsigned voff, unsigned dst) {
    unsigned sv;
    if constexpr (NT) asm volatile("s_mov_b32 %0, m0\n\ts_mov_b32 m0, %3\n\ts_nop 0\n\tglobal_load_lds_dwordx4 %1, %2 nt\n\ts_mov_b32 m0, %0" : "=&s"(sv) : "v"(voff), "s"(sbase), "s"(dst) : "memory");
    else asm volatile("s_mov_b32 %0, m0\n\ts_mov_b32 m0, %3\n\ts_nop 0\n\tglobal_load_lds_dwordx4 %1, %2\n\ts_mov_b32 m0, %0" : "=&s"(sv) : "v"(voff), "s"(sbase), "s"(dst) : "memory");
}
#define dma_nt dma_<KV_NT>
#define dma dma_<W_NT>
DI float swapmax(float x) { auto r = __builtin_amdgcn_permlane32_swap(__float_as_uint(x), __float_as_uint(x), false, false); return __builtin_fmaxf(__uint_as_float(r[0]), __uint_as_float(r[1])); }
DI float swapadd(float x) { auto r = __builtin_amdgcn_permlane32_swap(__float_as_uint(x), __float_as_uint(x), false, false); return __uint_as_float(r[0]) + __uint_as_float(r[1]); }
#define MX3(a, b, c) __builtin_fmaxf(__builtin_fmaxf((a), (b)), (c))
DI void st_sc1(__amdgpu_buffer_rsrc_t r, unsigned off, u32x4 v) { __builtin_amdgcn_raw_buffer_store_b128(v, r, off, 0, 16); }
DI u32x4 ld_sc1(__amdgpu_buffer_rsrc_t r, unsigned off) { return __builtin_amdgcn_raw_buffer_load_b128(r, off, 0, 16); }
DI void wait_flag(GAS u64* f, u64 tag) {
    for (unsigned spin = 0; spin < (1u << 20); ++spin) {
        const u64 v = __hip_atomic_load(f, RLX_AGENT);
        if (__all(v == tag)) break;
        __builtin_amdgcn_s_sleep(1);
    }
    asm volatile("" ::: "memory");
}

#define STAMP(i) do {} while (0)

constexpr size_t WS_EPOCH = 0;
constexpr size_t WS_QFLAG = 4096;
constexpr size_t WS_F1 = 32768;
constexpr size_t WS_F2 = 196608;
constexpr size_t WS_Q = 1u << 20;
constexpr size_t WS_P1 = 8u << 20;
constexpr size_t WS_P2 = 20u << 20;
constexpr size_t WS_STAMP = 32u << 20;

struct QSched { int needW[37], needX[10]; };
constexpr int QNW = 7;
constexpr QSched make_qsched() {
    QSched s{}; int posW[36] = {}, posX[9] = {}; int n = 0;
    posX[0] = n++; for (int i = 0; i < 4; ++i) posW[i] = n++;
    posX[1] = n++; for (int i = 4; i < QNW; ++i) posW[i] = n++;
    posX[2] = n++;
    s.needW[0] = n - 1 - posW[0]; s.needX[0] = n - 1 - posX[0];
    for (int ks = 0; ks < 36; ++ks) {
        if (ks + 1 < 36) { s.needW[ks + 1] = n - 1 - posW[ks + 1]; if ((ks + 1) % 4 == 0) s.needX[(ks + 1) / 4] = n - 1 - posX[(ks + 1) / 4]; }
        if (ks + QNW < 36) posW[ks + QNW] = n++; else if (ks < 33) n++;
        if (ks % 4 == 0 && ks / 4 + 3 < 9) posX[ks / 4 + 3] = n++;
    }
    return s;
}
template <int N> DI void vmcnt_c() { asm volatile("s_waitcnt vmcnt(%0)" :: "i"(N) : "memory"); }
template <int I, int E, class F> DI void static_for(F&& f) { if constexpr (I < E) { f(std::integral_constant<int, I>{}); static_for<I + 1, E>(f); } }

__global__ __launch_bounds__(512, 2) void gqa_fused(const char* __restrict__ X, const char* __restrict__ Kc, const char* __restrict__ Vc,
                                                    const char* __restrict__ Wq, const char* __restrict__ Wo, char* __restrict__ Out, char* __restrict__ ws) {
    extern __shared__ __attribute__((aligned(16))) char smem[];
    const unsigned tid = threadIdx.x, lane = tid & 63;
    const unsigned wid = __builtin_amdgcn_readfirstlane(tid >> 6);
    const unsigned r32 = lane & 31, hi = lane >> 5, w4 = wid & 3, g = wid >> 2;
    const unsigned L0 = (unsigned)(uintptr_t)smem;
    const unsigned wg = blockIdx.x, xcd = wg & 7, slot = wg >> 3;
    const unsigned kvh = 2 * xcd + (slot & 1), sp = (slot >> 1) & 1, b = slot >> 2;
    const unsigned pair = b * HKV + kvh, me = pair * 2 + sp;
    STAMP(0);
    const char* Kb = Kc + ((size_t)pair * SK + sp * 1024) * 256;
    const char* Vb = Vc + ((size_t)pair * SK + sp * 1024) * 256;
    const unsigned kvoff = (lane >> 4) * 256 + ((((lane & 15) ^ (lane >> 4)) << 4) ^ (w4 << 6)) + w4 * 1024;
    const unsigned vvoff = (lane >> 4) * 256 + (((lane & 15) ^ ((lane >> 4) << 2)) << 4) + w4 * 1024;
    auto dmaK1 = [&](unsigned t, unsigned sl, unsigned j) { dma_nt(Kb + t * TILEB + j * 4096, kvoff, L0 + KR + sl * TILEB + w4 * 1024 + j * 4096); };
    auto dmaV1 = [&](unsigned t, unsigned sl, unsigned j) { dma_nt(Vb + t * TILEB + j * 4096, vvoff, L0 + VR + sl * TILEB + w4 * 1024 + j * 4096); };
    auto dmaK = [&](unsigned t, unsigned sl) { dmaK1(t, sl, 0); dmaK1(t, sl, 1); dmaK1(t, sl, 2); dmaK1(t, sl, 3); };
    auto dmaV = [&](unsigned t, unsigned sl) { dmaV1(t, sl, 0); dmaV1(t, sl, 1); dmaV1(t, sl, 2); dmaV1(t, sl, 3); };
    const __amdgpu_buffer_rsrc_t qrs = __builtin_amdgcn_make_buffer_rsrc(ws + WS_Q + (size_t)pair * 32768, 0, 32768, 0x00020000);
    GAS u64* qflag = (GAS u64*)(ws + WS_QFLAG + pair * 128);
    const unsigned g1 = (lane >> 4) & 1, q4 = (lane & 15) >> 2, p4 = lane & 3;

    bf16x8 qf[8];
    u64 tag;
    if (sp == 0) {
        tag = __hip_atomic_load((GAS u64*)(ws + WS_EPOCH + b * 256), RLX_AGENT) + 1;
        const unsigned phh = wid >> 2, pdb = wid & 3;
        constexpr unsigned XB = 2 * TILEB, WR = XB + 9 * 8192;
        constexpr QSched QS = make_qsched();
        const char* Xb = X + (size_t)b * SQ * DM * 2;
        const unsigned xvoff = (8 * wid + (lane >> 3)) * (DM * 2) + (((lane & 7) ^ (4 * (wid & 1) + (lane >> 4))) << 4);
        const char* Wb = Wq + ((2 * kvh + phh) * 128 + 32 * pdb) * 2;
        const unsigned wvoff = (lane >> 2) * 8192 + (lane & 3) * 16;
        const unsigned wr = L0 + WR + wid * (QNW * 1024);
        auto rot = [&](unsigned j) { const unsigned c = j + b; return c >= 9 ? c - 9 : c; };
        auto dX = [&](int j) { dma(Xb + rot(j) * 128, xvoff, L0 + XB + j * 8192 + wid * 1024); };
        auto dW = [&](int ks) { dma(Wb + (size_t)(rot(ks >> 2) * 4 + (ks & 3)) * 131072, wvoff, wr + (ks % QNW) * 1024); };
        dX(0); dW(0); dW(1); dW(2); dW(3); dX(1); dW(4); dW(5); dW(6); dX(2);
        const unsigned ps = ((p4 & 1) << 1) | (p4 >> 1);
        const unsigned trb = wr + (8 * hi + q4) * 64 + g1 * 32 + ps * 8;
        const unsigned xr0 = L0 + XB + r32 * 128 + ((hi ^ ((r32 >> 1) & 7)) << 4);
        f32x16 acc0 = {}, acc1 = {};
        bf16x8 fa[2], fb0[2], fb1[2];
        auto rd = [&](int ks) {
            fa[ks & 1] = cat(lds_tr(trb + (ks % QNW) * 1024), lds_tr(trb + (ks % QNW) * 1024 + 256));
            const unsigned xa = (xr0 ^ ((ks & 3) << 5)) + (ks >> 2) * 8192;
            fb0[ks & 1] = lds_b128(xa); fb1[ks & 1] = lds_b128(xa + 4096);
        };
        vmcnt_c<QS.needW[0]>();
        asm volatile("s_barrier" ::: "memory");
        STAMP(1);
        rd(0);
        static_for<0, 36>([&](auto ks_) {
            constexpr int ks = ks_;
            if constexpr (ks + 1 < 36) {
                if constexpr ((ks + 1) % 4 == 0) {
                    constexpr int n = QS.needW[ks + 1] < QS.needX[(ks + 1) / 4] ? QS.needW[ks + 1] : QS.needX[(ks + 1) / 4];
                    vmcnt_c<n>(); asm volatile("s_barrier" ::: "memory");
                } else vmcnt_c<QS.needW[ks + 1]>();
                rd(ks + 1);
            }
            SBAR();
            acc0 = MFMA(fa[ks & 1], fb0[ks & 1], acc0); acc1 = MFMA(fa[ks & 1], fb1[ks & 1], acc1);
            SBAR();
            if constexpr (ks + QNW < 36) dW(ks + QNW); else if constexpr (ks < 33) dmaK1(g, g, ks - 29);
            if constexpr (ks % 4 == 0 && ks / 4 + 3 < 9) dX(ks / 4 + 3);
            SBAR();
        });
        u32x4 f0[2], f1[2];
#pragma unroll
        for (int u = 0; u < 2; ++u) {
#pragma unroll
            for (int e = 0; e < 4; ++e) { f0[u][e] = cvtpk(acc0[8 * u + 2 * e] * QSCALE, acc0[8 * u + 2 * e + 1] * QSCALE); f1[u][e] = cvtpk(acc1[8 * u + 2 * e] * QSCALE, acc1[8 * u + 2 * e + 1] * QSCALE); }
            st_sc1(qrs, ((phh * 2 + 0) * 8 + 2 * pdb + u) * 1024 + lane * 16, f0[u]);
            st_sc1(qrs, ((phh * 2 + 1) * 8 + 2 * pdb + u) * 1024 + lane * 16, f1[u]);
        }
        BARRIER();
        STAMP(2);
#pragma unroll
        for (int u = 0; u < 2; ++u) {
            lds_w128(L0 + SCR + ((phh * 2 + 0) * 8 + 2 * pdb + u) * 1024 + lane * 16, f0[u]);
            lds_w128(L0 + SCR + ((phh * 2 + 1) * 8 + 2 * pdb + u) * 1024 + lane * 16, f1[u]);
        }
        VMCNT(0);
        if (lane == 0) __hip_atomic_store(qflag + wid, tag, RLX_AGENT);
        if (!g) { dmaK(2, 2); dmaV(0, 0); } else { dmaK(3, 3); dmaV(1, 1); }
        BARRIER();
#pragma unroll
        for (int s = 0; s < 8; ++s) qf[s] = lds_b128(L0 + SCR + (w4 * 8 + s) * 1024 + lane * 16);
        if (g) { dmaK(4, 4); dmaV(2, 2); }
    } else {
        if (!g) { dmaK(0, 0); dmaK(2, 2); dmaK(3, 3); dmaK(4, 4); } else { dmaK(1, 1); dmaV(0, 0); dmaV(1, 1); dmaV(2, 2); }
        tag = __hip_atomic_load((GAS u64*)(ws + WS_EPOCH + b * 256), RLX_AGENT) + 1;
        STAMP(1);
        wait_flag(qflag + (w4 >> 1) * 4 + (lane & 3), tag);
        STAMP(2);
#pragma unroll
        for (int s = 0; s < 8; ++s) qf[s] = __builtin_bit_cast(bf16x8, ld_sc1(qrs, (w4 * 8 + s) * 1024 + lane * 16));
        VMCNT(0);
        BARRIER();
    }
    STAMP(3);

    const unsigned kr0 = L0 + KR + r32 * 256 + ((hi ^ (r32 & 15)) << 4);
    const unsigned vr0 = L0 + VR + (4 * hi + q4) * 256 + (q4 << 6) + g1 * 32 + p4 * 8;
    const char* Wob = Wo + ((size_t)kvh * 256 * DM + sp * 288) * 2;
    const unsigned ovoff = (lane >> 2) * (DM * 2) + (lane & 3) * 16 + w4 * (16 * DM * 2);
    auto dmaO = [&](unsigned nb, unsigned dst) {
#pragma unroll
        for (int j = 0; j < 4; ++j) dma(Wob + nb * 64 + j * (64 * DM * 2), ovoff, L0 + dst + w4 * 1024 + j * 4096);
    };
    f32x16 o[4] = {};
    f32x16 s0, s1, negm = {};
    u32x4 pf[4];
    float m = 0.f, l = 0.f;

    constexpr int LA = LOOKA;
    auto rdK = [&](unsigned ka, int i) { return lds_b128((ka ^ ((i >> 1) << 5)) + (i & 1) * 8192); };
    auto rdV = [&](unsigned va, int i) { const unsigned a = (va ^ ((i & 3) << 6)) + (i >> 2) * 4096; return cat(lds_tr(a), lds_tr(a + 2048)); };
    auto MPH = [&](auto doqk_, auto dopv_, unsigned ksl_, unsigned vsl_) {
        constexpr bool DQ = decltype(doqk_)::value, DP = decltype(dopv_)::value;
        constexpr int N = 16 * (int)DQ + 16 * (int)DP, OFFV = DQ ? 16 : 0;
        const unsigned ka = kr0 + ksl_ * TILEB, va = vr0 + vsl_ * TILEB;
        bf16x8 f[N];
        auto rd = [&](int i) { if (DQ && i < 16) f[i] = rdK(ka, i); else f[i] = rdV(va, i - OFFV); };
#pragma unroll
        for (int i = 0; i < LA; ++i) rd(i);
        SBAR();
#pragma unroll
        for (int i = 0; i < N; ++i) {
            if (i + LA < N) rd(i + LA);
            if (DQ && i < 16) {
                if (i == 0) s0 = MFMA(f[i], qf[0], negm); else if (i == 1) s1 = MFMA(f[i], qf[0], negm);
                else if (i & 1) s1 = MFMA(f[i], qf[i >> 1], s1); else s0 = MFMA(f[i], qf[i >> 1], s0);
            } else { const int j = i - OFFV; o[j & 3] = MFMA(f[i], __builtin_bit_cast(bf16x8, pf[j >> 2]), o[j & 3]); }
            SBAR();
        }
    };
    using T_ = std::integral_constant<bool, true>; using F_ = std::integral_constant<bool, false>;
    auto SM = [&](auto first_) {
        constexpr bool first = decltype(first_)::value;
        float a = MX3(s0[0], s0[1], s1[0]), c = MX3(s0[2], s0[3], s1[1]); a = MX3(a, s1[2], s1[3]);
#pragma unroll
        for (int r = 4; r < 16; r += 4) { a = MX3(a, s0[r], s0[r + 1]); c = MX3(c, s0[r + 2], s0[r + 3]); a = MX3(a, s1[r], s1[r + 1]); c = MX3(c, s1[r + 2], s1[r + 3]); }
        const float rm = swapmax(__builtin_fmaxf(a, c));
        if (first || __builtin_expect(__any(rm > THR), 0)) {
            const float dl = first ? rm : __builtin_fmaxf(rm, 0.f);
            const float f = first ? 0.f : __builtin_amdgcn_exp2f(-dl);
            m += dl; l *= f;
#pragma unroll
            for (int r = 0; r < 16; ++r) { s0[r] -= dl; s1[r] -= dl; negm[r] = -m; }
#pragma unroll
            for (int db = 0; db < 4; ++db) if (!first) o[db] *= f;
        }
        float sa = 0.f, sb = 0.f;
#pragma unroll
        for (int r = 0; r < 16; ++r) { s0[r] = __builtin_amdgcn_exp2f(s0[r]); s1[r] = __builtin_amdgcn_exp2f(s1[r]); }
        sa = s0[0]; sb = s1[0];
#pragma unroll
        for (int r = 1; r < 16; ++r) { sa += s0[r]; sb += s1[r]; }
        l += sa + sb;
#pragma unroll
        for (int e = 0; e < 4; ++e) { pf[0][e] = cvtpk(s0[2 * e], s0[2 * e + 1]); pf[1][e] = cvtpk(s0[8 + 2 * e], s0[9 + 2 * e]); pf[2][e] = cvtpk(s1[2 * e], s1[2 * e + 1]); pf[3][e] = cvtpk(s1[8 + 2 * e], s1[9 + 2 * e]); }
        asm volatile("" : "+v"(pf[0]), "+v"(pf[1]), "+v"(pf[2]), "+v"(pf[3]), "+v"(l));
    };
#define TICK(acc) do {} while (0)
    auto inc2 = [](unsigned s) { return s >= 3 ? s - 3 : s + 2; };
    if (g) { SBAR(); BARRIER(); STAMP(9); SBAR(); }
    unsigned t = g;
    unsigned kq = g, vq = g;
    auto ISSUE = [&]() {
        if (t <= 10) { dmaK(t + 5, kq); dmaV(t + 3, vq >= 2 ? vq - 2 : vq + 3); }
        else if (t <= 12) { dmaV(t + 3, vq >= 2 ? vq - 2 : vq + 3); dmaO(t - 11, KR + kq * TILEB); }
        else if (t == 13) { dmaO(2, KR + 3 * TILEB); dmaO(3, VR + 1 * TILEB); }
        else if (t == 14) { dmaO(8, KR + 4 * TILEB); dmaO(4, VR + 2 * TILEB); }
        else { dmaO(5, KR + 0 * TILEB); VMCNT(4); }
    };
    MPH(T_{}, F_{}, kq, 0);
    SBAR(); VMCNT(8); BARRIER(); STAMP(9 + t); SBAR();
    SM(T_{}); SBAR();
    dmaK(t + 5, kq); dmaV(t + 3, vq + 3);
    if (g) VMCNT(16); else VMCNT(8);
    SBAR(); BARRIER(); STAMP(10 + t); SBAR();
    for (int it = 0; it < 7; ++it) {
        kq = inc2(kq);
        MPH(T_{}, T_{}, kq, vq); TICK(cM);
        vq = inc2(vq); t += 2;
        SBAR(); VMCNT(8); TICK(cV); BARRIER(); STAMP(9 + t); SBAR(); TICK(cW1);
        SM(F_{}); SBAR(); TICK(cS);
        ISSUE(); TICK(cD);
        SBAR(); BARRIER(); STAMP(10 + t); SBAR(); TICK(cW2);
    }
    MPH(F_{}, T_{}, 0, vq);
    l = swapadd(l);

    const unsigned wor = (4 * hi + q4) * 64 + g1 * 32 + p4 * 8;
    bf16x8 wf[16], wf8[16];
    auto rdWo = [&](bf16x8* dst, unsigned blk) {
#pragma unroll
        for (int ks = 0; ks < 16; ++ks) dst[ks] = cat(lds_tr(L0 + blk + wor + ks * 1024), lds_tr(L0 + blk + wor + ks * 1024 + 512));
    };
    const unsigned scr = L0 + SCR + w4 * 8192 + lane * 16;
    const unsigned lsel = L0 + VR + 0 * TILEB;
    (void)lsel;
    if (!g) {
        SBAR(); VMCNT(4); BARRIER(); STAMP(25); SBAR();
        const float inv = 1.f / l;
#pragma unroll
        for (int db = 0; db < 4; ++db)
#pragma unroll
            for (int u = 0; u < 2; ++u) {
                u32x4 f;
#pragma unroll
                for (int e = 0; e < 4; ++e) f[e] = cvtpk(o[db][8 * u + 2 * e] * inv, o[db][8 * u + 2 * e + 1] * inv);
                lds_w128(scr + (db * 2 + u) * 1024, f);
            }
        {
            const unsigned blk = w4 == 0 ? KR + 1 * TILEB : w4 == 1 ? KR + 2 * TILEB : w4 == 2 ? KR + 3 * TILEB : VR + 1 * TILEB;
            rdWo(wf, blk);
            if (w4 < 2) rdWo(wf8, KR + 4 * TILEB);
            asm volatile("s_waitcnt lgkmcnt(0)" ::: "memory");
            *(LAS float*)(uintptr_t)(L0 + blk + lane * 4) = m + __builtin_amdgcn_logf(l);
        }
        STAMP(4);
        BARRIER();
        dmaO(6, VR + 0 * TILEB); dmaO(7, KR + 4 * TILEB);
        VMCNT(0);
        BARRIER();
        BARRIER();
    } else {
        STAMP(4);
        BARRIER();
        const unsigned blkA = w4 == 0 ? KR + 1 * TILEB : w4 == 1 ? KR + 2 * TILEB : w4 == 2 ? KR + 3 * TILEB : VR + 1 * TILEB;
        const float lseA = *(const LAS float*)(uintptr_t)(L0 + blkA + lane * 4);
        const float lseB = m + __builtin_amdgcn_logf(l);
        const float mx = __builtin_fmaxf(lseA, lseB), ea = __builtin_amdgcn_exp2f(lseA - mx), eb = __builtin_amdgcn_exp2f(lseB - mx);
        const float den = ea + eb, ca = ea / den, cb = eb / (den * l);
        const float lse = mx + __builtin_amdgcn_logf(den);
        const __amdgpu_buffer_rsrc_t p1o = __builtin_amdgcn_make_buffer_rsrc(ws + WS_P1 + (size_t)(me * 4 + w4) * 9216, 0, 9216, 0x00020000);
        const __amdgpu_buffer_rsrc_t p1i = __builtin_amdgcn_make_buffer_rsrc(ws + WS_P1 + (size_t)((me ^ 1) * 4 + w4) * 9216, 0, 9216, 0x00020000);
#pragma unroll
        for (int db = 0; db < 4; ++db)
#pragma unroll
            for (int u = 0; u < 2; ++u) {
                const u32x4 a = *(const LAS u32x4*)(uintptr_t)(scr + (db * 2 + u) * 1024);
                u32x4 f;
#pragma unroll
                for (int e = 0; e < 4; ++e) {
                    o[db][8 * u + 2 * e] = o[db][8 * u + 2 * e] * cb + bflo(a[e]) * ca; o[db][8 * u + 2 * e + 1] = o[db][8 * u + 2 * e + 1] * cb + bfhi(a[e]) * ca;
                    f[e] = cvtpk(o[db][8 * u + 2 * e], o[db][8 * u + 2 * e + 1]);
                }
                st_sc1(p1o, (db * 2 + u) * 1024 + lane * 16, f);
            }
        __builtin_amdgcn_raw_buffer_store_b32(__float_as_uint(lse), p1o, 8192 + lane * 4, 0, 16);
        VMCNT(0);
        if (lane == 0) __hip_atomic_store((GAS u64*)(ws + WS_F1 + (me * 4 + w4) * 128), tag, RLX_AGENT);
        STAMP(5);
        BARRIER();
        rdWo(wf, w4 == 0 ? VR + 2 * TILEB : w4 == 1 ? KR + 0 * TILEB : w4 == 2 ? VR + 0 * TILEB : KR + 4 * TILEB);
        wait_flag((GAS u64*)(ws + WS_F1 + ((me ^ 1) * 4 + w4) * 128), tag);
        STAMP(26);
        u32x4 rp[8];
#pragma unroll
        for (int j = 0; j < 8; ++j) rp[j] = ld_sc1(p1i, j * 1024 + lane * 16);
        const float lseR = __uint_as_float(__builtin_amdgcn_raw_buffer_load_b32(p1i, 8192 + lane * 4, 0, 16));
        const float mx2 = __builtin_fmaxf(lse, lseR), e0 = __builtin_amdgcn_exp2f(lse - mx2), e1 = __builtin_amdgcn_exp2f(lseR - mx2);
        const float c0 = e0 / (e0 + e1), c1 = e1 / (e0 + e1);
#pragma unroll
        for (int db = 0; db < 4; ++db)
#pragma unroll
            for (int u = 0; u < 2; ++u) {
                u32x4 f;
#pragma unroll
                for (int e = 0; e < 4; ++e) f[e] = cvtpk(o[db][8 * u + 2 * e] * c0 + bflo(rp[db * 2 + u][e]) * c1, o[db][8 * u + 2 * e + 1] * c0 + bfhi(rp[db * 2 + u][e]) * c1);
                lds_w128(scr + (db * 2 + u) * 1024, f);
            }
        BARRIER();
    }
    STAMP(27);
    {
        f32x16 c0 = {}, c1 = {}, c8 = {};
#pragma unroll
        for (int ks = 0; ks < 16; ++ks) {
            const unsigned a = L0 + SCR + ((ks >> 3) * 16 + (ks & 7)) * 1024 + lane * 16;
            const bf16x8 b0 = lds_b128(a), b1 = lds_b128(a + 8192);
            c0 = MFMA(wf[ks], b0, c0); c1 = MFMA(wf[ks], b1, c1);
            if (wid < 2) c8 = MFMA(wf8[ks], wid ? b1 : b0, c8);
        }
        const __amdgpu_buffer_rsrc_t p2 = __builtin_amdgcn_make_buffer_rsrc(ws + WS_P2 + (size_t)(b * 2 + sp) * 16 * 36864, 0, 16 * 36864, 0x00020000);
        auto put = [&](const f32x16& c, unsigned piece) {
#pragma unroll
            for (int u = 0; u < 2; ++u) {
                u32x4 f;
#pragma unroll
                for (int e = 0; e < 4; ++e) f[e] = cvtpk(c[8 * u + 2 * e], c[8 * u + 2 * e + 1]);
                st_sc1(p2, kvh * 36864 + (piece + u) * 1024 + lane * 16, f);
            }
        };
        put(c0, wid * 4); put(c1, wid * 4 + 2);
        if (wid < 2) put(c8, 32 + wid * 2);
        VMCNT(0);
        asm volatile("s_barrier" ::: "memory");
        STAMP(28);
        if (wid < 3) {
            GAS u64* f2 = (GAS u64*)(ws + WS_F2);
            if (wid == 0 && lane < 16) __hip_atomic_store(f2 + (size_t)((b * HKV + lane) * 2 + sp) * 16 + kvh, tag, RLX_AGENT);
            wait_flag(f2 + (size_t)me * 16 + (lane & 15), tag);
            STAMP(29);
            if (wid == 0 && kvh == 0 && sp == 0 && lane == 0) __hip_atomic_store((GAS u64*)(ws + WS_EPOCH + b * 256), tag, RLX_AGENT);
            if (tid < 144) {
                const unsigned c = kvh * 144 + tid;
                u32x4 v[16];
#pragma unroll
                for (int s = 0; s < 16; ++s) v[s] = ld_sc1(p2, s * 36864 + c * 16);
                float acc[8] = {};
#pragma unroll
                for (int s = 0; s < 16; ++s)
#pragma unroll
                    for (int e = 0; e < 4; ++e) { acc[2 * e] += bflo(v[s][e]); acc[2 * e + 1] += bfhi(v[s][e]); }
                const unsigned piece = c >> 6, ln = c & 63, nb = piece >> 2, rb = (piece >> 1) & 1, u = piece & 1;
                char* op = Out + ((size_t)(b * 64 + rb * 32 + (ln & 31)) * DM + sp * 288 + nb * 32 + u * 16 + (ln >> 5) * 4) * 2;
                *(GAS u32x2*)op = u32x2{cvtpk(acc[0], acc[1]), cvtpk(acc[2], acc[3])};
                *(GAS u32x2*)(op + 16) = u32x2{cvtpk(acc[4], acc[5]), cvtpk(acc[6], acc[7])};
            }
            STAMP(30);
        }
    }
}

extern "C" void kernel_launch(void* const* d_in, const int* in_sizes, int n_in, void* d_out, int out_size, void* d_ws, size_t ws_size, hipStream_t stream) {
    (void)hipFuncSetAttribute((const void*)gqa_fused, hipFuncAttributeMaxDynamicSharedMemorySize, LDS_BYTES);
    hipLaunchKernelGGL(gqa_fused, dim3(256), dim3(512), LDS_BYTES, stream, (const char*)d_in[0], (const char*)d_in[1], (const char*)d_in[2], (const char*)d_in[3],
                       (const char*)d_in[4], (char*)d_out, (char*)d_ws);
}
```

```cpp
#include <hip/hip_runtime.h>
#include <stdint.h>
#include <type_traits>

#define KV_NT 1
#define PRIO 1
#define KVMIX 0
#define PF_CODE 0
#define LOOKA 6

#define DI __device__ __forceinline__
#define LAS __attribute__((address_space(3)))
#define GAS __attribute__((address_space(1)))
typedef __bf16 bf16x8 __attribute__((ext_vector_type(8)));
typedef __bf16 bf16x2 __attribute__((ext_vector_type(2)));
typedef short s16x4 __attribute__((ext_vector_type(4)));
typedef short s16x8 __attribute__((ext_vector_type(8)));
typedef float f32x2 __attribute__((ext_vector_type(2)));
typedef float f32x16 __attribute__((ext_vector_type(16)));
typedef unsigned u32x4 __attribute__((ext_vector_type(4)));
typedef unsigned u32x2 __attribute__((ext_vector_type(2)));
typedef unsigned long long u64;

constexpr int SQ = 64, DM = 576, HKV = 16, SK = 2048;
constexpr unsigned TILEB = 16384;
constexpr unsigned KR = 0, VR = 5 * TILEB;
constexpr unsigned SCR = 8 * TILEB;
constexpr unsigned LDS_BYTES = 10 * TILEB;
constexpr float QSCALE = 0.08838834764831845f * 1.4426950408889634f;

#define AI __attribute__((always_inline))
#define SBAR() __builtin_amdgcn_sched_barrier(0)
#define MFMA(a, b, c) __builtin_amdgcn_mfma_f32_32x32x16_bf16((a), (b), (c), 0, 0, 0)
#define VMCNT(n) asm volatile("s_waitcnt vmcnt(%0)" :: "i"(n) : "memory")
#define BARRIER() asm volatile("s_waitcnt lgkmcnt(0)\n\ts_barrier" ::: "memory")
#define BAR0() asm volatile("s_barrier" ::: "memory")
#define RLX_AGENT __ATOMIC_RELAXED, __HIP_MEMORY_SCOPE_AGENT

DI unsigned cvtpk(float lo, float hi) { f32x2 v = {lo, hi}; bf16x2 b = __builtin_convertvector(v, bf16x2); return __builtin_bit_cast(unsigned, b); }
DI float bflo(unsigned u) { return __uint_as_float(u << 16); }
DI float bfhi(unsigned u) { return __uint_as_float(u & 0xffff0000u); }
DI bf16x8 lds_b128(unsigned a) { return *(const LAS bf16x8*)(uintptr_t)a; }
DI s16x4 lds_tr(unsigned a) { return __builtin_amdgcn_ds_read_tr16_b64_v4i16((LAS s16x4*)(uintptr_t)a); }
DI bf16x8 cat(s16x4 lo, s16x4 hi) { s16x8 r = __builtin_shufflevector(lo, hi, 0, 1, 2, 3, 4, 5, 6, 7); return __builtin_bit_cast(bf16x8, r); }
DI void lds_w128(unsigned a, u32x4 v) { *(LAS u32x4*)(uintptr_t)a = v; }
template <int NT> DI void dma_(const void* sbase, unsigned voff, unsigned dst) {
    unsigned sv;
    if constexpr (NT) asm volatile("s_mov_b32 %0, m0\n\ts_mov_b32 m0, %3\n\ts_nop 0\n\tglobal_load_lds_dwordx4 %1, %2 nt\n\ts_mov_b32 m0, %0" : "=&s"(sv) : "v"(voff), "s"(sbase), "s"(dst) : "memory");
    else asm volatile("s_mov_b32 %0, m0\n\ts_mov_b32 m0, %3\n\ts_nop 0\n\tglobal_load_lds_dwordx4 %1, %2\n\ts_mov_b32 m0, %0" : "=&s"(sv) : "v"(voff), "s"(sbase), "s"(dst) : "memory");
}
DI f32x16 mfma_c(bf16x8 a, bf16x8 b, const f32x16& c) {
    f32x16 d; asm("v_mfma_f32_32x32x16_bf16 %0, %1, %2, %3" : "=&v"(d) : "v"(a), "v"(b), "v"(c)); return d; }
DI float fsub_s(float a, float b) { float r; asm("v_sub_f32_e32 %0, %1, %2" : "=v"(r) : "v"(a), "v"(b)); return r; }
DI float swapmax(float x) { auto r = __builtin_amdgcn_permlane32_swap(__float_as_uint(x), __float_as_uint(x), false, false); return __builtin_fmaxf(__uint_as_float(r[0]), __uint_as_float(r[1])); }
DI float swapadd(float x) { auto r = __builtin_amdgcn_permlane32_swap(__float_as_uint(x), __float_as_uint(x), false, false); return __uint_as_float(r[0]) + __uint_as_float(r[1]); }
#define MX3(a, b, c) __builtin_fmaxf(__builtin_fmaxf((a), (b)), (c))
DI void st_sc1(__amdgpu_buffer_rsrc_t r, unsigned off, u32x4 v) { __builtin_amdgcn_raw_buffer_store_b128(v, r, off, 0, 16); }
DI u32x4 ld_sc1(__amdgpu_buffer_rsrc_t r, unsigned off) { return __builtin_amdgcn_raw_buffer_load_b128(r, off, 0, 16); }
DI void wait_flag(GAS u64* f, u64 tag) {
#pragma unroll 1
    for (unsigned spin = 0; spin < (1u << 20); ++spin) {
        const u64 v = __hip_atomic_load(f, RLX_AGENT);
        if (__all(v == tag)) break;
        __builtin_amdgcn_s_sleep(1);
    }
    asm volatile("" ::: "memory");
}

#define STAMP(i) do {} while (0)

#define WS_BASE 0
constexpr size_t WS_EPOCH = 0;
constexpr size_t WS_F1 = 65536;
constexpr size_t WS_F2 = 262144;
constexpr size_t WS_P1 = 8u << 20;
constexpr size_t WS_P2 = 20u << 20;
constexpr size_t WS_STAMP = 32u << 20;

using T_ = std::integral_constant<bool, true>; using F_ = std::integral_constant<bool, false>;

__global__ __launch_bounds__(512, 2) void gqa_fused(const char* X, const char* Kc, const char* Vc, const char* Wq, const char* Wo, char* Out, char* ws) {
    extern __shared__ __attribute__((aligned(16))) char smem[];
    asm volatile("" : "+s"(X), "+s"(Kc), "+s"(Vc), "+s"(Wq), "+s"(Wo), "+s"(Out), "+s"(ws));
    const unsigned tid = threadIdx.x, lane = tid & 63;
    const unsigned wid = __builtin_amdgcn_readfirstlane(tid >> 6);
    const unsigned r32 = lane & 31, hi = lane >> 5, w4 = wid & 3, g = wid >> 2;
    const unsigned L0 = (unsigned)(uintptr_t)smem;
    const unsigned wg = blockIdx.x, xcd = wg & 7, slot = wg >> 3;
    const unsigned kvh = 2 * xcd + (slot & 1), sp = (slot >> 1) & 1, b = slot >> 2;
    const unsigned pair = b * HKV + kvh, me = pair * 2 + sp;
    STAMP(0);
#define TICK(acc) do {} while (0)
    const char* Kb = Kc + ((size_t)pair * SK + sp * 1024) * 256;
    const char* Vb = Vc + ((size_t)pair * SK + sp * 1024) * 256;
    const unsigned kvoff = (lane >> 4) * 256 + ((((lane & 15) ^ (lane >> 4)) << 4) ^ (w4 << 6)) + w4 * 1024;
    const unsigned vvoff = (lane >> 4) * 256 + (((lane & 15) ^ ((lane >> 4) << 2)) << 4) + w4 * 1024;
    const unsigned Lw = L0 + w4 * 1024;
    auto dmaK1 = [&](unsigned t, unsigned sl, unsigned j) { dma_<KV_NT>(Kb + t * TILEB + j * 4096, kvoff, Lw + KR + sl * TILEB + j * 4096); };
    auto dmaV1 = [&](unsigned t, unsigned sl, unsigned j) { dma_<KV_NT>(Vb + t * TILEB + j * 4096, vvoff, Lw + VR + sl * TILEB + j * 4096); };
    auto dmaK = [&](unsigned t, unsigned sl) { dmaK1(t, sl, 0); dmaK1(t, sl, 1); dmaK1(t, sl, 2); dmaK1(t, sl, 3); };
    auto dmaV = [&](unsigned t, unsigned sl) { dmaV1(t, sl, 0); dmaV1(t, sl, 1); dmaV1(t, sl, 2); dmaV1(t, sl, 3); };
    const unsigned g1 = (lane >> 4) & 1, q4 = (lane & 15) >> 2, p4 = lane & 3;

    bf16x8 qf[8];
    u64 tag;
    auto load_tag = [&]() { const char* p = ws + WS_EPOCH + b * 256; asm volatile("" : "+s"(p)); tag = *(const __attribute__((address_space(4))) u64*)p; };
#define TAG_READY() do { asm volatile("" : "+s"(tag)); tag += 1; } while (0)
    auto touch = [&](const void* sbase, unsigned voff, unsigned dst) {
        unsigned sv;
        asm volatile("s_mov_b32 %0, m0\n\ts_mov_b32 m0, %3\n\ts_nop 0\n\tglobal_load_lds_dword %1, %2\n\ts_mov_b32 m0, %0" : "=&s"(sv) : "v"(voff), "s"(sbase), "s"(dst) : "memory");
    };
    {
        constexpr unsigned XR = 3 * TILEB, WR = 6 * TILEB;
        if (g) {
            const char* Xb = X + (size_t)b * SQ * DM * 2;
            const unsigned xvoff = (8 * w4 + (lane >> 3)) * (DM * 2) + (((lane & 7) ^ (4 * (w4 & 1) + (lane >> 4))) << 4);
            const char* Wb = Wq + (2 * kvh + (w4 >> 1)) * 256 + (size_t)(w4 & 1) * 16 * 8192;
            const unsigned wvoff = (lane >> 4) * 8192 + (((lane & 15) ^ ((lane >> 4) << 2)) << 4);
            unsigned cb = b + (kvh & 1) * 4 + sp * 2; cb = cb >= 9 ? cb - 9 : cb;
            unsigned xb = cb;
            auto xhalf = [&](unsigned j, unsigned h) { dma_<0>(Xb + xb * 128 + h * (32 * DM * 2), xvoff, Lw + XR + (j & 3) * 8192 + h * 4096); if (h) xb = xb == 8 ? 0 : xb + 1; };
            auto phase = [&](unsigned p) {
                xhalf((p + 1) >> 1, (p & 1) ^ 1);
#pragma unroll
                for (int j = 0; j < 4; ++j) dma_<0>(Wb + (size_t)(cb * 2 + (p & 1)) * (32 * 8192) + j * 32768, wvoff, L0 + WR + (p & 3) * TILEB + w4 * 4096 + j * 1024);
                if (p & 1) cb = cb == 8 ? 0 : cb + 1;
            };
            xhalf(0, 0);
            phase(0); phase(1); phase(2);
            load_tag();
            VMCNT(10); BAR0();
            STAMP(1);
            phase(3);
#pragma unroll 1
            for (unsigned p = 4; p < 18; ++p) { VMCNT(10); BAR0(); phase(p); }
            VMCNT(10); BAR0(); dmaK(0, 0);
            VMCNT(9); BAR0(); dmaK(1, 1);
            VMCNT(8); BAR0(); dmaK(2, 2);
            BAR0();
            STAMP(2);
            dmaV(0, 0);
            TAG_READY();
            VMCNT(12); BAR0();
#pragma unroll
            for (int s = 0; s < 8; ++s) qf[s] = lds_b128(L0 + SCR + (w4 * 8 + s) * 1024 + lane * 16);
            SBAR();
            dmaK(3, 3); dmaV(1, 1); dmaK(4, 4); dmaV(2, 2);
            VMCNT(24);
        } else {
            load_tag();
            const unsigned ps = ((p4 & 1) << 1) | (p4 >> 1);
            const unsigned wa0 = L0 + WR + (w4 >> 1) * 8192 + (8 * hi + q4) * 256 + (q4 << 6) + g1 * 32 + ps * 8;
            const unsigned xa0 = L0 + XR + (w4 & 1) * 4096 + r32 * 128 + ((hi ^ ((r32 >> 1) & 7)) << 4);
            f32x16 acc[4] = {};
            bf16x8 fa[2][8], fb[2][2];
            auto rdA = [&](int set, unsigned p, int i) { const unsigned a = ((wa0 + (p & 3) * TILEB) ^ ((i & 3) << 6)) + (i >> 2) * 4096; fa[set][i] = cat(lds_tr(a), lds_tr(a + 1024)); };
            auto rdB = [&](int set, unsigned p, int k) { fb[set][k] = lds_b128((xa0 + ((p >> 1) & 3) * 8192) ^ ((2 * set + k) << 5)); };
            auto step = [&](auto set_, auto last_, unsigned pn) AI {
                constexpr int set = decltype(set_)::value; constexpr bool last = decltype(last_)::value;
                SBAR(); BARRIER(); SBAR();
#pragma unroll
                for (int i = 0; i < 8; ++i) {
                    acc[i & 3] = MFMA(fa[set][i], fb[set][i >> 2], acc[i & 3]);
                    if (!last) { if (i < 4) { rdA(set ^ 1, pn, 2 * i); rdA(set ^ 1, pn, 2 * i + 1); } else if (i == 4) { rdB(set ^ 1, pn, 0); rdB(set ^ 1, pn, 1); } }
                    SBAR();
                }
            };
            using I0 = std::integral_constant<int, 0>; using I1 = std::integral_constant<int, 1>;
            TAG_READY();
            BAR0();
            STAMP(1);
            rdB(0, 0, 0); rdB(0, 0, 1);
#pragma unroll
            for (int i = 0; i < 8; ++i) rdA(0, 0, i);
#pragma unroll 1
            for (unsigned p = 0; p < 16; p += 2) { step(I0{}, F_{}, p + 1); step(I1{}, F_{}, p + 2); }
            step(I0{}, F_{}, 17); step(I1{}, T_{}, 0);
            STAMP(2);
#pragma unroll
            for (int s = 0; s < 8; ++s) {
                u32x4 f;
#pragma unroll
                for (int e = 0; e < 4; ++e) f[e] = cvtpk(acc[s >> 1][8 * (s & 1) + 2 * e] * QSCALE, acc[s >> 1][8 * (s & 1) + 2 * e + 1] * QSCALE);
                qf[s] = __builtin_bit_cast(bf16x8, f);
                lds_w128(L0 + SCR + (w4 * 8 + s) * 1024 + lane * 16, f);
            }
            BARRIER();
        }
    }
    STAMP(3);

    const unsigned kr0 = L0 + KR + r32 * 256 + ((hi ^ (r32 & 15)) << 4);
    const unsigned vr0 = L0 + VR + (4 * hi + q4) * 256 + (q4 << 6) + g1 * 32 + p4 * 8;
    const char* Wob = Wo + ((size_t)kvh * 256 * DM + sp * 288) * 2;
    const unsigned ovoff = (lane >> 2) * (DM * 2) + (lane & 3) * 16 + w4 * (16 * DM * 2);
    auto pcol = [&](unsigned nb) { return sp ? (nb == 8 ? 0u : nb + 1) : nb; };
    auto dmaO1 = [&](unsigned nb, unsigned dst, int j) { dma_<0>(Wob + pcol(nb) * 64 + j * (64 * DM * 2), ovoff, Lw + dst + j * 4096); };
    auto dmaO = [&](unsigned nb, unsigned dst) { dmaO1(nb, dst, 0); dmaO1(nb, dst, 1); dmaO1(nb, dst, 2); dmaO1(nb, dst, 3); };
    f32x16 o[4] = {};
    f32x16 s0, s1, negm = {};
    u32x4 pf[4];
    float m = 0.f, l = 0.f;

    constexpr int LA = LOOKA;
    auto rdK = [&](unsigned ka, int i) { return lds_b128((ka ^ ((i >> 1) << 5)) + (i & 1) * 8192); };
    auto rdV = [&](unsigned va, int i) { const unsigned a = (va ^ ((i & 3) << 6)) + (i >> 2) * 4096; return cat(lds_tr(a), lds_tr(a + 2048)); };
    auto MPH = [&](auto doqk_, auto dopv_, unsigned ksl_, unsigned vsl_) AI {
        constexpr bool DQ = decltype(doqk_)::value, DP = decltype(dopv_)::value;
        constexpr int N = 16 * (int)DQ + 16 * (int)DP, OFFV = DQ ? 16 : 0;
        const unsigned ka = kr0 + ksl_ * TILEB, va = vr0 + vsl_ * TILEB;
        bf16x8 f[N];
        auto rd = [&](int i) { if (DQ && i < 16) f[i] = rdK(ka, i); else f[i] = rdV(va, i - OFFV); };
        if (PRIO) __builtin_amdgcn_s_setprio(PRIO);
#pragma unroll
        for (int i = 0; i < LA; ++i) rd(i);
        SBAR();
#pragma unroll
        for (int i = 0; i < N; ++i) {
            if (i + LA < N) rd(i + LA);
            if (DQ && i < 16) {
                if (i == 0) s0 = mfma_c(f[i], qf[0], negm); else if (i == 1) s1 = mfma_c(f[i], qf[0], negm);
                else if (i & 1) s1 = MFMA(f[i], qf[i >> 1], s1); else s0 = MFMA(f[i], qf[i >> 1], s0);
            } else { const int j = i - OFFV; o[j & 3] = MFMA(f[i], __builtin_bit_cast(bf16x8, pf[j >> 2]), o[j & 3]); }
            SBAR();
        }
        if (PRIO) __builtin_amdgcn_s_setprio(0);
    };
    auto RESCALE = [&](auto first_) AI {
        constexpr bool first = decltype(first_)::value;
        float a = MX3(s0[0], s0[1], s1[0]), c = MX3(s0[2], s0[3], s1[1]); a = MX3(a, s1[2], s1[3]);
#pragma unroll
        for (int r = 4; r < 16; r += 4) { a = MX3(a, s0[r], s0[r + 1]); c = MX3(c, s0[r + 2], s0[r + 3]); a = MX3(a, s1[r], s1[r + 1]); c = MX3(c, s1[r + 2], s1[r + 3]); }
        const float rm = swapmax(__builtin_fmaxf(a, c));
        const float dl = first ? rm : __builtin_fmaxf(rm, 0.f);
        m += dl;
#pragma unroll
        for (int r = 0; r < 16; ++r) { s0[r] = fsub_s(s0[r], dl); s1[r] = fsub_s(s1[r], dl); negm[r] = fsub_s(negm[r], dl); }
        if (!first) {
            const float f = __builtin_amdgcn_exp2f(-dl);
            l *= f;
#pragma unroll
            for (int db = 0; db < 4; ++db) o[db] *= f;
        }
    };
    auto EXPS = [&](auto&& hook) AI {
        float sa = 0.f, sb = 0.f;
#pragma unroll
        for (int k = 0; k < 8; ++k) {
            const f32x16& s = k < 4 ? s0 : s1; const int r0 = (k & 3) * 4;
            const float e0 = __builtin_amdgcn_exp2f(s[r0]), e1 = __builtin_amdgcn_exp2f(s[r0 + 1]), e2 = __builtin_amdgcn_exp2f(s[r0 + 2]), e3 = __builtin_amdgcn_exp2f(s[r0 + 3]);
            if (k == 0) { sa = e0 + e2; sb = e1 + e3; } else { sa += e0; sb += e1; sa += e2; sb += e3; }
            unsigned w0 = cvtpk(e0, e1), w1 = cvtpk(e2, e3);
            asm volatile("" : "+v"(w0), "+v"(w1), "+v"(sa), "+v"(sb));
            pf[k >> 1][(k & 1) * 2] = w0; pf[k >> 1][(k & 1) * 2 + 1] = w1;
            SBAR(); hook(k); SBAR();
        }
        return sa + sb;
    };
    auto SM = [&](auto first_, auto&& hook) AI {
        constexpr bool first = decltype(first_)::value;
        if (first) RESCALE(first_);
        float ts = EXPS(hook);
        if (!first && __builtin_expect(__any(!(ts <= 256.f)), 0)) { RESCALE(F_{}); ts = EXPS([](int) {}); }
        l += ts;
        asm volatile("" : "+v"(l));
    };
    auto inc2 = [](unsigned s) { return s >= 3 ? s - 3 : s + 2; };
    unsigned t = g;
    unsigned kq = g, vq = g;
    auto vnext = [&]() { return vq >= 2 ? vq - 2 : vq + 3; };
    auto hookKV = [&](int j) { if (j < 4) dmaK1(t + 5, kq, j); else dmaV1(t + 3, vnext(), j - 4); };
    if (g) { SBAR(); BARRIER(); STAMP(9); SBAR(); }
    MPH(T_{}, F_{}, kq, 0);
    SBAR();
    if (g) VMCNT(16);
    BARRIER(); STAMP(9 + t); SBAR();
    SM(T_{}, hookKV); SBAR();
    VMCNT(16);
    SBAR(); BARRIER(); STAMP(10 + t); SBAR();
#pragma unroll 1
    while (t <= 8) {
        kq = inc2(kq);
        MPH(T_{}, T_{}, kq, vq); TICK(cM);
        vq = inc2(vq); t += 2;
        SBAR(); VMCNT(8); TICK(cV); BARRIER(); STAMP(9 + t); SBAR(); TICK(cW1);
        SM(F_{}, hookKV); SBAR(); TICK(cS);
        SBAR(); BARRIER(); STAMP(10 + t); SBAR(); TICK(cW2);
    }
#pragma unroll 1
    while (t <= 13) {
        kq = inc2(kq);
        MPH(T_{}, T_{}, kq, vq);
        vq = inc2(vq); t += 2;
        SBAR(); if (t == 13) VMCNT(4); else if (t == 14) VMCNT(12); else VMCNT(8);
        BARRIER(); STAMP(9 + t); SBAR();
        if (t <= 12) dmaV(t + 3, vnext());
        const unsigned nb0 = 2 * (t - 12), d0 = t == 12 ? KR + TILEB : KR + kq * TILEB, d1 = t == 12 ? KR + 2 * TILEB : VR + vnext() * TILEB;
        SBAR();
        SM(F_{}, [&](int j) { if (t >= 12) dmaO1(nb0 + (j & 1), (j & 1) ? d1 : d0, j >> 1); });
        SBAR();
        if (t == 15) VMCNT(8);
        SBAR(); BARRIER(); STAMP(10 + t); SBAR();
    }
    MPH(F_{}, T_{}, 0, vq);
    l = swapadd(l);

    const unsigned wor = (4 * hi + q4) * 64 + g1 * 32 + p4 * 8;
    bf16x8 wf[16];
    auto rdWo = [&](bf16x8* dst, unsigned blk) {
#pragma unroll
        for (int ks = 0; ks < 16; ++ks) dst[ks] = cat(lds_tr(L0 + blk + wor + ks * 1024), lds_tr(L0 + blk + wor + ks * 1024 + 512));
    };
    auto xslot = [](unsigned w) { return w == 3 ? VR + 1 * TILEB : KR + (w + 1) * TILEB; };
    const unsigned scr = L0 + xslot(w4) + lane * 16;
    if (!g) {
        const float inv = 1.f / l;
        float lseA = m + __builtin_amdgcn_logf(l);
        u32x4 f[8];
#pragma unroll
        for (int j = 0; j < 8; ++j) {
#pragma unroll
            for (int e = 0; e < 4; ++e) f[j][e] = cvtpk(o[j >> 1][8 * (j & 1) + 2 * e] * inv, o[j >> 1][8 * (j & 1) + 2 * e + 1] * inv);
            asm volatile("" : "+v"(f[j]));
        }
        asm volatile("" : "+v"(lseA));
        SBAR(); VMCNT(8); BARRIER(); STAMP(25); SBAR();
        rdWo(wf, xslot(w4));
        asm volatile("s_waitcnt lgkmcnt(0)" ::: "memory");
#pragma unroll
        for (int j = 0; j < 8; ++j) lds_w128(scr + j * 1024, f[j]);
        *(LAS float*)(uintptr_t)(L0 + xslot(w4) + 8192 + lane * 4) = lseA;
        dmaO(8, VR + 4 * TILEB);
        STAMP(4);
        BARRIER();
        VMCNT(0);
        BARRIER();
        BARRIER();
    } else {
        const float lseB = m + __builtin_amdgcn_logf(l), invl = 1.f / l;
        const __amdgpu_buffer_rsrc_t p1o = __builtin_amdgcn_make_buffer_rsrc(ws + WS_P1 + (size_t)(me * 4 + w4) * 9216, 0, 9216, 0x00020000);
        const __amdgpu_buffer_rsrc_t p1i = __builtin_amdgcn_make_buffer_rsrc(ws + WS_P1 + (size_t)((me ^ 1) * 4 + w4) * 9216, 0, 9216, 0x00020000);
        STAMP(4);
        BARRIER();
        const float lseA = *(const LAS float*)(uintptr_t)(L0 + xslot(w4) + 8192 + lane * 4);
        u32x4 pa[8];
#pragma unroll
        for (int j = 0; j < 8; ++j) pa[j] = *(const LAS u32x4*)(uintptr_t)(scr + j * 1024);
        const float mx = __builtin_fmaxf(lseA, lseB), ea = __builtin_amdgcn_exp2f(lseA - mx), eb = __builtin_amdgcn_exp2f(lseB - mx);
        const float den = ea + eb, rden = __builtin_amdgcn_rcpf(den), ca = ea * rden, cb = eb * rden * invl;
        const float lse = mx + __builtin_amdgcn_logf(den);
        __builtin_amdgcn_raw_buffer_store_b32(__float_as_uint(lse), p1o, 8192 + lane * 4, 0, 16);
#pragma unroll
        for (int j = 0; j < 8; ++j) {
            u32x4 f;
#pragma unroll
            for (int e = 0; e < 4; ++e) {
                const int r = 8 * (j & 1) + 2 * e;
                o[j >> 1][r] = o[j >> 1][r] * cb + bflo(pa[j][e]) * ca; o[j >> 1][r + 1] = o[j >> 1][r + 1] * cb + bfhi(pa[j][e]) * ca;
                f[e] = cvtpk(o[j >> 1][r], o[j >> 1][r + 1]);
            }
            st_sc1(p1o, j * 1024 + lane * 16, f);
        }
        VMCNT(0);
        if (lane == 0) __hip_atomic_store((GAS u64*)(ws + WS_F1 + (me * 4 + w4) * 128), tag, RLX_AGENT);
        STAMP(5);
        BARRIER();
        rdWo(wf, w4 == 0 ? KR + 4 * TILEB : w4 == 1 ? VR + 2 * TILEB : w4 == 2 ? KR + 0 * TILEB : VR + 3 * TILEB);
        wait_flag((GAS u64*)(ws + WS_F1 + ((me ^ 1) * 4 + w4) * 128), tag);
        STAMP(26);
        const float lseR = __uint_as_float(__builtin_amdgcn_raw_buffer_load_b32(p1i, 8192 + lane * 4, 0, 16));
        u32x4 rp[8];
#pragma unroll
        for (int j = 0; j < 8; ++j) rp[j] = ld_sc1(p1i, j * 1024 + lane * 16);
        const float mx2 = __builtin_fmaxf(lse, lseR), e0 = __builtin_amdgcn_exp2f(lse - mx2), e1 = __builtin_amdgcn_exp2f(lseR - mx2);
        const float rd2 = __builtin_amdgcn_rcpf(e0 + e1), c0 = e0 * rd2, c1 = e1 * rd2;
#pragma unroll
        for (int j = 0; j < 8; ++j) {
            u32x4 f;
#pragma unroll
            for (int e = 0; e < 4; ++e) { const int r = 8 * (j & 1) + 2 * e; f[e] = cvtpk(o[j >> 1][r] * c0 + bflo(rp[j][e]) * c1, o[j >> 1][r + 1] * c0 + bfhi(rp[j][e]) * c1); }
            lds_w128(scr + j * 1024, f);
        }
        BARRIER();
    }
    STAMP(27);
    {
        const __amdgpu_buffer_rsrc_t p2 = __builtin_amdgcn_make_buffer_rsrc(ws + WS_P2 + (size_t)(b * 2 + sp) * 16 * 36864, 0, 16 * 36864, 0x00020000);
        auto put = [&](const f32x16& c, unsigned piece) {
#pragma unroll
            for (int u = 0; u < 2; ++u) {
                u32x4 f;
#pragma unroll
                for (int e = 0; e < 4; ++e) f[e] = cvtpk(c[8 * u + 2 * e], c[8 * u + 2 * e + 1]);
                st_sc1(p2, kvh * 36864 + (piece + u) * 1024 + lane * 16, f);
            }
        };
        constexpr int LB = 6;
        const unsigned rbf = wid & 1;
        const unsigned ob[4] = {L0 + xslot(rbf) + lane * 16, L0 + xslot(2 + rbf) + lane * 16, L0 + xslot(rbf ^ 1) + lane * 16, L0 + xslot(2 + (rbf ^ 1)) + lane * 16};
        {
            bf16x8 f[32]; f32x16 c = {}, c2 = {};
            auto rd = [&](int i) { f[i] = lds_b128(ob[i >> 3] + (i & 7) * 1024); };
#pragma unroll
            for (int i = 0; i < LB; ++i) rd(i);
            SBAR();
#pragma unroll
            for (int i = 0; i < 32; ++i) {
                if (i + LB < 32) rd(i + LB);
                if (i < 16) c = MFMA(wf[i], f[i], c); else c2 = MFMA(wf[i - 16], f[i], c2);
                if (i == 19) put(c, wid * 4 + rbf * 2);
                SBAR();
            }
            if (wid < 2) {
                bf16x8 fa[16]; c = f32x16{};
                auto rd8 = [&](int i) { const unsigned a = L0 + VR + 4 * TILEB + wor + i * 1024; fa[i] = cat(lds_tr(a), lds_tr(a + 512)); f[i] = lds_b128(ob[i >> 3] + (i & 7) * 1024); };
#pragma unroll
                for (int i = 0; i < 4; ++i) rd8(i);
                SBAR();
#pragma unroll
                for (int i = 0; i < 16; ++i) {
                    if (i + 4 < 16) rd8(i + 4);
                    c = MFMA(fa[i], f[i], c);
                    if (i == 3) put(c2, wid * 4 + (rbf ^ 1) * 2);
                    SBAR();
                }
                put(c, 32 + wid * 2);
            } else put(c2, wid * 4 + (rbf ^ 1) * 2);
        }
        VMCNT(0);
        GAS u64* f2 = (GAS u64*)(ws + WS_F2);
        {
            const unsigned cons = lane < 2 ? wid * 2 + lane : wid * 8 + (lane - 2);
            if (lane < (wid < 2 ? 10u : 2u)) __hip_atomic_store(f2 + (size_t)((b * HKV + cons) * 2 + sp) * 32 + (lane < 2 ? 0 : 16) + kvh, tag, RLX_AGENT);
        }
        STAMP(28);
        if (wid >= 5) {
            const unsigned w3 = wid - 5;
            wait_flag(f2 + (size_t)me * 32 + (w3 == 2 ? 16 : 0) + (lane & 15), tag);
            STAMP(29);
            if (w3 == 0 && kvh == 0 && sp == 0 && lane == 0) __hip_atomic_store((GAS u64*)(ws + WS_EPOCH + b * 256), tag, RLX_AGENT);
            if (w3 < 2 || lane < 16) {
                const unsigned cc = w3 < 2 ? kvh * 128 + w3 * 64 + lane : 2048 + kvh * 16 + lane;
                u32x4 v[16];
#pragma unroll
                for (int s = 0; s < 16; ++s) v[s] = ld_sc1(p2, s * 36864 + cc * 16);
                float acc[8] = {};
#pragma unroll
                for (int s = 0; s < 16; ++s)
#pragma unroll
                    for (int e = 0; e < 4; ++e) { acc[2 * e] += bflo(v[s][e]); acc[2 * e + 1] += bfhi(v[s][e]); }
                const unsigned piece = cc >> 6, ln = cc & 63, nb = piece >> 2, rb = (piece >> 1) & 1, u = piece & 1;
                char* op = Out + ((size_t)(b * 64 + rb * 32 + (ln & 31)) * DM + sp * 288 + pcol(nb) * 32 + u * 16 + (ln >> 5) * 4) * 2;
                *(GAS u32x2*)op = u32x2{cvtpk(acc[0], acc[1]), cvtpk(acc[2], acc[3])};
                *(GAS u32x2*)(op + 16) = u32x2{cvtpk(acc[4], acc[5]), cvtpk(acc[6], acc[7])};
            }
            STAMP(30);
        }
    }
}

extern "C" void kernel_launch(void* const* d_in, const int* in_sizes, int n_in, void* d_out, int out_size, void* d_ws, size_t ws_size, hipStream_t stream) {
    (void)hipFuncSetAttribute((const void*)gqa_fused, hipFuncAttributeMaxDynamicSharedMemorySize, LDS_BYTES);
    hipLaunchKernelGGL(gqa_fused, dim3(256), dim3(512), LDS_BYTES, stream, (const char*)d_in[0], (const char*)d_in[1], (const char*)d_in[2], (const char*)d_in[3],
                       (const char*)d_in[4], (char*)d_out, (char*)d_ws + WS_BASE);
}
```

```cpp
#include <hip/hip_runtime.h>
#include <stdint.h>
#include <type_traits>

#define KV_NT 1
#define PRIO 1
#define PF_CODE 24000
#define NUMA 0
#define PF_PAGE 0
#define POLL1 1
#define LOOKA 6

#define DI __device__ __forceinline__
#define LAS __attribute__((address_space(3)))
#define GAS __attribute__((address_space(1)))
typedef __bf16 bf16x8 __attribute__((ext_vector_type(8)));
typedef __bf16 bf16x2 __attribute__((ext_vector_type(2)));
typedef short s16x4 __attribute__((ext_vector_type(4)));
typedef short s16x8 __attribute__((ext_vector_type(8)));
typedef float f32x2 __attribute__((ext_vector_type(2)));
typedef float f32x16 __attribute__((ext_vector_type(16)));
typedef unsigned u32x4 __attribute__((ext_vector_type(4)));
typedef unsigned u32x2 __attribute__((ext_vector_type(2)));
typedef unsigned long long u64;

constexpr int SQ = 64, DM = 576, HKV = 16, SK = 2048;
constexpr unsigned TILEB = 16384;
constexpr unsigned KR = 0, VR = 5 * TILEB;
constexpr unsigned SCR = 8 * TILEB;
constexpr unsigned LDS_BYTES = 10 * TILEB;
constexpr float QSCALE = 0.08838834764831845f * 1.4426950408889634f;

#define AI __attribute__((always_inline))
#define SBAR() __builtin_amdgcn_sched_barrier(0)
#define MFMA(a, b, c) __builtin_amdgcn_mfma_f32_32x32x16_bf16((a), (b), (c), 0, 0, 0)
#define VMCNT(n) asm volatile("s_waitcnt vmcnt(%0)" :: "i"(n) : "memory")
#define BARRIER() asm volatile("s_waitcnt lgkmcnt(0)\n\ts_barrier" ::: "memory")
#define BAR0() asm volatile("s_barrier" ::: "memory")
#define RLX_AGENT __ATOMIC_RELAXED, __HIP_MEMORY_SCOPE_AGENT

DI unsigned cvtpk(float lo, float hi) { f32x2 v = {lo, hi}; bf16x2 b = __builtin_convertvector(v, bf16x2); return __builtin_bit_cast(unsigned, b); }
DI float bflo(unsigned u) { return __uint_as_float(u << 16); }
DI float bfhi(unsigned u) { return __uint_as_float(u & 0xffff0000u); }
DI bf16x8 lds_b128(unsigned a) { return *(const LAS bf16x8*)(uintptr_t)a; }
DI s16x4 lds_tr(unsigned a) { return __builtin_amdgcn_ds_read_tr16_b64_v4i16((LAS s16x4*)(uintptr_t)a); }
DI bf16x8 cat(s16x4 lo, s16x4 hi) { s16x8 r = __builtin_shufflevector(lo, hi, 0, 1, 2, 3, 4, 5, 6, 7); return __builtin_bit_cast(bf16x8, r); }
DI void lds_w128(unsigned a, u32x4 v) { *(LAS u32x4*)(uintptr_t)a = v; }
template <int NT, int OFF> DI void dma1(const void* sbase, unsigned voff, unsigned m) {
    if constexpr (NT) asm volatile("s_mov_b32 m0, %2\n\ts_nop 0\n\tglobal_load_lds_dwordx4 %0, %1 offset:%3 nt" :: "v"(voff), "s"(sbase), "s"(m), "i"(OFF) : "memory");
    else asm volatile("s_mov_b32 m0, %2\n\ts_nop 0\n\tglobal_load_lds_dwordx4 %0, %1 offset:%3" :: "v"(voff), "s"(sbase), "s"(m), "i"(OFF) : "memory");
}
template <int NT, int OFF> DI void dma2(const void* sbase, unsigned v0, unsigned v1, unsigned m) {
    if constexpr (NT) asm volatile("s_mov_b32 m0, %3\n\ts_nop 0\n\tglobal_load_lds_dwordx4 %0, %2 offset:%4 nt\n\tglobal_load_lds_dwordx4 %1, %2 offset:%5 nt" :: "v"(v0), "v"(v1), "s"(sbase), "s"(m), "i"(OFF), "i"(OFF + 1024) : "memory");
    else asm volatile("s_mov_b32 m0, %3\n\ts_nop 0\n\tglobal_load_lds_dwordx4 %0, %2 offset:%4\n\tglobal_load_lds_dwordx4 %1, %2 offset:%5" :: "v"(v0), "v"(v1), "s"(sbase), "s"(m), "i"(OFF), "i"(OFF + 1024) : "memory");
}
DI f32x16 mfma_c(bf16x8 a, bf16x8 b, const f32x16& c) {
    f32x16 d; asm("v_mfma_f32_32x32x16_bf16 %0, %1, %2, %3" : "=&v"(d) : "v"(a), "v"(b), "v"(c)); return d; }
DI float fsub_s(float a, float b) { float r; asm("v_sub_f32_e32 %0, %1, %2" : "=v"(r) : "v"(a), "v"(b)); return r; }
DI float swapmax(float x) { auto r = __builtin_amdgcn_permlane32_swap(__float_as_uint(x), __float_as_uint(x), false, false); return __builtin_fmaxf(__uint_as_float(r[0]), __uint_as_float(r[1])); }
DI float swapadd(float x) { auto r = __builtin_amdgcn_permlane32_swap(__float_as_uint(x), __float_as_uint(x), false, false); return __uint_as_float(r[0]) + __uint_as_float(r[1]); }
#define MX3(a, b, c) __builtin_fmaxf(__builtin_fmaxf((a), (b)), (c))
DI void st_sc1(__amdgpu_buffer_rsrc_t r, unsigned off, u32x4 v) { __builtin_amdgcn_raw_buffer_store_b128(v, r, off, 0, 16); }
DI u32x4 ld_sc1(__amdgpu_buffer_rsrc_t r, unsigned off) { return __builtin_amdgcn_raw_buffer_load_b128(r, off, 0, 16); }
DI void wait_flag(GAS u64* f, u64 tag) {
#pragma unroll 1
    for (unsigned spin = 0; spin < (1u << 20); ++spin) {
        const u64 v = __hip_atomic_load(f, RLX_AGENT);
        if (__all(v == tag)) break;
        __builtin_amdgcn_s_sleep(1);
    }
    asm volatile("" ::: "memory");
}

#define STAMP(i) do {} while (0)

#define WS_BASE 0
constexpr size_t WS_EPOCH = 0;
constexpr size_t WS_F1 = 65536;
constexpr size_t WS_F2 = 262144;
constexpr size_t WS_P1 = 8u << 20;
constexpr size_t WS_P2 = (20u << 20) + 256;
constexpr unsigned P2STRIDE = 16 * 36864 + 256;
constexpr size_t WS_STAMP = 32u << 20;

using T_ = std::integral_constant<bool, true>; using F_ = std::integral_constant<bool, false>;

__global__ __launch_bounds__(512, 2) void gqa_fused(const char* X, const char* Kc, const char* Vc, const char* Wq, const char* Wo, char* Out, char* ws) {
    extern __shared__ __attribute__((aligned(16))) char smem[];
    asm volatile("" : "+s"(X), "+s"(Kc), "+s"(Vc), "+s"(Wq), "+s"(Wo), "+s"(Out), "+s"(ws));
    u64 pc0; asm volatile("s_getpc_b64 %0" : "=s"(pc0));
    const unsigned tid = threadIdx.x, lane = tid & 63;
    const unsigned wid = __builtin_amdgcn_readfirstlane(tid >> 6);
    const unsigned r32 = lane & 31, hi = lane >> 5, w4 = wid & 3, g = wid >> 2;
    const unsigned L0 = (unsigned)(uintptr_t)smem;
    const unsigned wg = blockIdx.x, xcd = wg & 7, slot = wg >> 3;
    const unsigned kvh = (xcd & 3) * 4 + (slot & 3), sp = xcd >> 2, b = slot >> 2;
    const unsigned pair = b * HKV + kvh, me = pair * 2 + sp;
    STAMP(0);
    const unsigned par = sp;
    constexpr unsigned TS = 16384;
    const unsigned woff = par * 262144 + w4 * 4096;
    const char* Kb = Kc + (size_t)pair * SK * 256 + woff;
    const char* Vb = Vc + (size_t)pair * SK * 256 + woff;
    const unsigned kv0 = (lane >> 4) * 256 + (((lane & 15) ^ (lane >> 4)) << 4), kv1 = kv0 ^ 64, kv2 = kv0 ^ 128, kv3 = kv0 ^ 192;
    const unsigned vvoff = (lane >> 4) * 256 + (((lane & 15) ^ ((lane >> 4) << 2)) << 4);
    const unsigned Lw = L0 + w4 * 1024, Lq = L0 + w4 * 4096;
    auto dmaK = [&](unsigned t, unsigned sl) { dma2<KV_NT, 0>(Kb + t * TS, kv0, kv1, Lq + KR + sl * TILEB); dma2<KV_NT, 2048>(Kb + t * TS, kv2, kv3, Lq + KR + sl * TILEB); };
    auto dmaV = [&](unsigned t, unsigned sl) { dma2<KV_NT, 0>(Vb + t * TS, vvoff, vvoff, Lq + VR + sl * TILEB); dma2<KV_NT, 2048>(Vb + t * TS, vvoff, vvoff, Lq + VR + sl * TILEB); };
    const unsigned g1 = (lane >> 4) & 1, q4 = (lane & 15) >> 2, p4 = lane & 3;

    bf16x8 qf[8];
    u64 tag;
    auto load_tag = [&]() { const char* p = ws + WS_EPOCH + b * 256; asm volatile("" : "+s"(p)); tag = *(const __attribute__((address_space(4))) u64*)p; };
#define TAG_READY() do { asm volatile("" : "+s"(tag)); tag += 1; } while (0)
    auto touch = [&](const void* sbase, unsigned voff, unsigned dst) {
        asm volatile("s_mov_b32 m0, %2\n\ts_nop 0\n\tglobal_load_lds_dword %0, %1" :: "v"(voff), "s"(sbase), "s"(dst) : "memory");
    };
    {
        constexpr unsigned XR = 3 * TILEB, WR = 6 * TILEB;
        if (g) {
            const char* Xb = X + (size_t)b * SQ * DM * 2;
            const unsigned xvoff = (8 * w4 + (lane >> 3)) * (DM * 2) + (((lane & 7) ^ (4 * (w4 & 1) + (lane >> 4))) << 4);
            const char* Wb = Wq + (2 * kvh + (w4 >> 1)) * 256 + (size_t)(w4 & 1) * 16 * 8192;
            const unsigned wvoff = (lane >> 4) * 8192 + (((lane & 15) ^ ((lane >> 4) << 2)) << 4);
            unsigned cb = b + (kvh & 3) * 2; cb = cb >= 9 ? cb - 9 : cb;
            unsigned xb = cb;
            auto xhalf = [&](unsigned j, unsigned h) { dma1<0, 0>(Xb + xb * 128 + h * (32 * DM * 2), xvoff, Lw + XR + (j & 3) * 8192 + h * 4096); if (h) xb = xb == 8 ? 0 : xb + 1; };
            auto phase = [&](unsigned p) {
                xhalf((p + 1) >> 1, (p & 1) ^ 1);
                const char* src = Wb + (size_t)(cb * 2 + (p & 1)) * (32 * 8192);
                dma2<0, 0>(src, wvoff, wvoff + 31744, Lq + WR + (p & 3) * TILEB); dma2<0, 2048>(src, wvoff + 2 * 31744, wvoff + 3 * 31744, Lq + WR + (p & 3) * TILEB);
                if (p & 1) cb = cb == 8 ? 0 : cb + 1;
            };
            xhalf(0, 0);
            phase(0); phase(1); phase(2);
            load_tag();
            VMCNT(10); BAR0();
            STAMP(1);
            phase(3);
#pragma unroll 1
            for (unsigned p = 4; p < 18; ++p) { VMCNT(10); BAR0(); phase(p); }
            VMCNT(10); BAR0(); dmaK(0, 0);
            VMCNT(9); BAR0(); dmaK(1, 1);
            VMCNT(8); BAR0(); dmaK(2, 2);
            BAR0();
            STAMP(2);
            dmaV(0, 0);
            TAG_READY();
            VMCNT(12); BAR0();
#pragma unroll
            for (int s = 0; s < 8; ++s) qf[s] = lds_b128(L0 + SCR + (w4 * 8 + s) * 1024 + lane * 16);
            SBAR();
            dmaK(3, 3); dmaV(1, 1); dmaK(4, 4); dmaV(2, 2);
            VMCNT(24);
        } else {
            if (PF_CODE && w4 == 0) { const unsigned ln = slot * 8 + (lane & 7); touch((const void*)(pc0 & ~127ull), ln * 128 < PF_CODE ? ln * 128 : 0u, Lw + VR); }
            if (PF_PAGE) {
                const char* p0 = w4 == 0 ? Kb : w4 == 1 ? ws + WS_P1 + me * 36864 + 8960 : w4 == 2 ? ws + WS_P2 + (b * 2 + sp) * P2STRIDE + 16 * 36864 : Wo + (size_t)kvh * 256 * DM * 2;
                const char* p1 = w4 == 0 ? Vb : w4 == 1 ? ws + WS_P1 + (me ^ 1) * 36864 + 8960 : w4 == 2 ? ws + WS_P2 + (b * 2 + sp) * P2STRIDE - 128 : Wo + (size_t)(kvh * 256 + 255) * DM * 2;
                const char* p2 = w4 == 0 ? Kb + 8 * TS : w4 == 1 ? Vb + 8 * TS : w4 == 2 ? Out + (size_t)b * SQ * DM * 2 : Out + (size_t)(b * SQ + 63) * DM * 2;
                unsigned d0, d1, d2;
                asm volatile("s_load_dword %0, %3, 0x0\n\ts_load_dword %1, %4, 0x0\n\ts_load_dword %2, %5, 0x0\n\ts_waitcnt lgkmcnt(0)" : "=&s"(d0), "=&s"(d1), "=&s"(d2) : "s"(p0), "s"(p1), "s"(p2) : "memory");
            }
            load_tag();
            const unsigned ps = ((p4 & 1) << 1) | (p4 >> 1);
            const unsigned wa0 = L0 + WR + (w4 >> 1) * 8192 + (8 * hi + q4) * 256 + (q4 << 6) + g1 * 32 + ps * 8;
            const unsigned xa0 = L0 + XR + (w4 & 1) * 4096 + r32 * 128 + ((hi ^ ((r32 >> 1) & 7)) << 4);
            f32x16 acc[4] = {};
            bf16x8 fa[2][8], fb[2][2];
            auto rdA = [&](int set, unsigned p, int i) { const unsigned a = ((wa0 + (p & 3) * TILEB) ^ ((i & 3) << 6)) + (i >> 2) * 4096; fa[set][i] = cat(lds_tr(a), lds_tr(a + 1024)); };
            auto rdB = [&](int set, unsigned p, int k) { fb[set][k] = lds_b128((xa0 + ((p >> 1) & 3) * 8192) ^ ((2 * set + k) << 5)); };
            auto step = [&](auto set_, auto last_, unsigned pn) AI {
                constexpr int set = decltype(set_)::value; constexpr bool last = decltype(last_)::value;
                SBAR(); BARRIER(); SBAR();
#pragma unroll
                for (int i = 0; i < 8; ++i) {
                    acc[i & 3] = MFMA(fa[set][i], fb[set][i >> 2], acc[i & 3]);
                    if (!last) { if (i < 4) { rdA(set ^ 1, pn, 2 * i); rdA(set ^ 1, pn, 2 * i + 1); } else if (i == 4) { rdB(set ^ 1, pn, 0); rdB(set ^ 1, pn, 1); } }
                    SBAR();
                }
            };
            using I0 = std::integral_constant<int, 0>; using I1 = std::integral_constant<int, 1>;
            TAG_READY();
            BAR0();
            STAMP(1);
            rdB(0, 0, 0); rdB(0, 0, 1);
#pragma unroll
            for (int i = 0; i < 8; ++i) rdA(0, 0, i);
#pragma unroll 1
            for (unsigned p = 0; p < 16; p += 2) { step(I0{}, F_{}, p + 1); step(I1{}, F_{}, p + 2); }
            step(I0{}, F_{}, 17); VMCNT(0); step(I1{}, T_{}, 0);
            STAMP(2);
#pragma unroll
            for (int s = 0; s < 8; ++s) {
                u32x4 f;
#pragma unroll
                for (int e = 0; e < 4; ++e) f[e] = cvtpk(acc[s >> 1][8 * (s & 1) + 2 * e] * QSCALE, acc[s >> 1][8 * (s & 1) + 2 * e + 1] * QSCALE);
                qf[s] = __builtin_bit_cast(bf16x8, f);
                lds_w128(L0 + SCR + (w4 * 8 + s) * 1024 + lane * 16, f);
            }
            BARRIER();
        }
    }
    STAMP(3);

    const unsigned kr0 = L0 + KR + r32 * 256 + ((hi ^ (r32 & 15)) << 4);
    const unsigned vr0 = L0 + VR + (4 * hi + q4) * 256 + (q4 << 6) + g1 * 32 + p4 * 8;
    const char* Wob = Wo + ((size_t)(kvh * 256 + w4 * 64) * DM + sp * 288) * 2;
    const unsigned ov0 = (lane >> 2) * (DM * 2) + (lane & 3) * 16;
    constexpr unsigned OVS = 16 * DM * 2 - 1024;
    auto pcol = [&](unsigned nb) { return sp ? (nb == 8 ? 0u : nb + 1) : nb; };
    auto dmaO1 = [&](const char* src, unsigned m, int j) {
        if (j == 0) dma1<0, 0>(src, ov0, m); else if (j == 1) dma1<0, 1024>(src, ov0 + OVS, m); else if (j == 2) dma1<0, 2048>(src, ov0 + 2 * OVS, m); else dma1<0, 3072>(src, ov0 + 3 * OVS, m);
    };
    f32x16 o[4] = {};
    f32x16 s0, s1, negm = {};
    u32x4 pf[4];
    float m = 0.f, l = 0.f;

    constexpr int LA = LOOKA;
    auto rdK = [&](unsigned ka, int i) { return lds_b128((ka ^ ((i >> 1) << 5)) + (i & 1) * 8192); };
    auto rdV = [&](unsigned va, int i) { const unsigned a = (va ^ ((i & 3) << 6)) + (i >> 2) * 4096; return cat(lds_tr(a), lds_tr(a + 2048)); };
    auto MPH = [&](auto doqk_, auto dopv_, unsigned ksl_, unsigned vsl_) AI {
        constexpr bool DQ = decltype(doqk_)::value, DP = decltype(dopv_)::value;
        constexpr int N = 16 * (int)DQ + 16 * (int)DP, OFFV = DQ ? 16 : 0;
        const unsigned ka = kr0 + ksl_ * TILEB, va = vr0 + vsl_ * TILEB;
        bf16x8 f[N];
        auto rd = [&](int i) { if (DQ && i < 16) f[i] = rdK(ka, i); else f[i] = rdV(va, i - OFFV); };
        if (PRIO) __builtin_amdgcn_s_setprio(PRIO);
#pragma unroll
        for (int i = 0; i < LA; ++i) rd(i);
        SBAR();
#pragma unroll
        for (int i = 0; i < N; ++i) {
            if (i + LA < N) rd(i + LA);
            if (DQ && i < 16) {
                if (i == 0) s0 = mfma_c(f[i], qf[0], negm); else if (i == 1) s1 = mfma_c(f[i], qf[0], negm);
                else if (i & 1) s1 = MFMA(f[i], qf[i >> 1], s1); else s0 = MFMA(f[i], qf[i >> 1], s0);
            } else { const int j = i - OFFV; o[j & 3] = MFMA(f[i], __builtin_bit_cast(bf16x8, pf[j >> 2]), o[j & 3]); }
            SBAR();
        }
        if (PRIO) __builtin_amdgcn_s_setprio(0);
    };
    auto RESCALE = [&](auto first_) AI {
        constexpr bool first = decltype(first_)::value;
        float a = MX3(s0[0], s0[1], s1[0]), c = MX3(s0[2], s0[3], s1[1]); a = MX3(a, s1[2], s1[3]);
#pragma unroll
        for (int r = 4; r < 16; r += 4) { a = MX3(a, s0[r], s0[r + 1]); c = MX3(c, s0[r + 2], s0[r + 3]); a = MX3(a, s1[r], s1[r + 1]); c = MX3(c, s1[r + 2], s1[r + 3]); }
        const float rm = swapmax(__builtin_fmaxf(a, c));
        const float dl = first ? rm : __builtin_fmaxf(rm, 0.f);
        m += dl;
#pragma unroll
        for (int r = 0; r < 16; ++r) { s0[r] = fsub_s(s0[r], dl); s1[r] = fsub_s(s1[r], dl); negm[r] = fsub_s(negm[r], dl); }
        if (!first) {
            const float f = __builtin_amdgcn_exp2f(-dl);
            l *= f;
#pragma unroll
            for (int db = 0; db < 4; ++db) o[db] *= f;
        }
    };
    auto EXPS = [&](auto&& hook) AI {
        float sa = 0.f, sb = 0.f;
#pragma unroll
        for (int k = 0; k < 8; ++k) {
            const f32x16& s = k < 4 ? s0 : s1; const int r0 = (k & 3) * 4;
            const float e0 = __builtin_amdgcn_exp2f(s[r0]), e1 = __builtin_amdgcn_exp2f(s[r0 + 1]), e2 = __builtin_amdgcn_exp2f(s[r0 + 2]), e3 = __builtin_amdgcn_exp2f(s[r0 + 3]);
            if (k == 0) { sa = e0 + e2; sb = e1 + e3; } else { sa += e0; sb += e1; sa += e2; sb += e3; }
            unsigned w0 = cvtpk(e0, e1), w1 = cvtpk(e2, e3);
            asm volatile("" : "+v"(w0), "+v"(w1), "+v"(sa), "+v"(sb));
            pf[k >> 1][(k & 1) * 2] = w0; pf[k >> 1][(k & 1) * 2 + 1] = w1;
            SBAR(); hook(k); SBAR();
        }
        return sa + sb;
    };
    auto SM = [&](auto first_, auto&& hook) AI {
        constexpr bool first = decltype(first_)::value;
        if (first) RESCALE(first_);
        float ts = EXPS(hook);
        if (!first && __builtin_expect(__any(!(ts <= 256.f)), 0)) { RESCALE(F_{}); ts = EXPS([](int) {}); }
        l += ts;
        asm volatile("" : "+v"(l));
    };
    auto inc2 = [](unsigned s) { return s >= 3 ? s - 3 : s + 2; };
    unsigned t = g;
    unsigned kq = g, vq = g;
    auto vnext = [&]() { return vq >= 2 ? vq - 2 : vq + 3; };
    const char* ksrc = Kb + (g + 5) * TS; const char* vsrc = Vb + (g + 3) * TS;
    unsigned mK, mV;
    auto hookKV = [&](int j) {
        if (j == 0) dma2<KV_NT, 0>(ksrc, kv0, kv1, mK); else if (j == 2) dma2<KV_NT, 2048>(ksrc, kv2, kv3, mK);
        else if (j == 4) dma2<KV_NT, 0>(vsrc, vvoff, vvoff, mV); else if (j == 6) dma2<KV_NT, 2048>(vsrc, vvoff, vvoff, mV);
    };
    auto prep = [&]() { mK = Lq + KR + kq * TILEB; mV = Lq + VR + vnext() * TILEB; asm volatile("" : "+s"(mK), "+s"(mV), "+s"(ksrc), "+s"(vsrc)); };
    if (g) { SBAR(); BARRIER(); STAMP(9); SBAR(); }
    prep();
    MPH(T_{}, F_{}, kq, 0);
    SBAR();
    if (g) VMCNT(16);
    BARRIER(); STAMP(9 + t); SBAR();
    SM(T_{}, hookKV); SBAR();
    VMCNT(16);
#pragma unroll 1
    while (t <= 8) {
        const unsigned vcur = vq;
        kq = inc2(kq); vq = inc2(vq); t += 2; ksrc += 2 * TS; vsrc += 2 * TS; prep();
        SBAR(); BARRIER(); STAMP(8 + t); SBAR();
        MPH(T_{}, T_{}, kq, vcur);
        SBAR(); VMCNT(8); BARRIER(); STAMP(9 + t); SBAR();
        SM(F_{}, hookKV); SBAR();
    }
#pragma unroll 1
    while (t <= 13) {
        const unsigned vcur = vq;
        kq = inc2(kq); vq = inc2(vq); t += 2; vsrc += 2 * TS; prep();
        const char* oA = Wob + pcol(t == 11 ? 8 : 2 * t - 26) * 64; const char* oB = Wob + pcol(2 * t - 25) * 64;
        asm volatile("" : "+s"(oA), "+s"(oB));
        SBAR(); BARRIER(); STAMP(8 + t); SBAR();
        MPH(T_{}, T_{}, kq, vcur);
        SBAR(); if (t == 14) VMCNT(4); else VMCNT(8);
        BARRIER(); STAMP(9 + t); SBAR();
        if (t <= 12) { hookKV(4); hookKV(6); }
        SBAR();
        SM(F_{}, [&](int j) { if (!(j & 1)) { if (t != 12) dmaO1(oA, mK, j >> 1); if (t >= 13) dmaO1(oB, mV, j >> 1); } }); SBAR();
        if (t == 15) VMCNT(8);
    }
    SBAR(); BARRIER(); STAMP(10 + t); SBAR();
    MPH(F_{}, T_{}, 0, vq);
    l = swapadd(l);

    const unsigned wor = (4 * hi + q4) * 64 + g1 * 32 + p4 * 8;
    bf16x8 wf[16];
    auto rdWo = [&](bf16x8* dst, unsigned blk) {
#pragma unroll
        for (int ks = 0; ks < 16; ++ks) dst[ks] = cat(lds_tr(L0 + blk + wor + ks * 1024), lds_tr(L0 + blk + wor + ks * 1024 + 512));
    };
    auto xslot = [](unsigned w) { return (w & 1) ? VR + (1 + (w >> 1)) * TILEB : KR + (3 + (w >> 1)) * TILEB; };
    const unsigned scr = L0 + xslot(w4) + lane * 16;
    if (!g) {
        const float inv = 1.f / l;
        float lseA = m + __builtin_amdgcn_logf(l);
        u32x4 f[8];
#pragma unroll
        for (int j = 0; j < 8; ++j) {
#pragma unroll
            for (int e = 0; e < 4; ++e) f[j][e] = cvtpk(o[j >> 1][8 * (j & 1) + 2 * e] * inv, o[j >> 1][8 * (j & 1) + 2 * e + 1] * inv);
            asm volatile("" : "+v"(f[j]));
        }
        asm volatile("" : "+v"(lseA));
        SBAR(); VMCNT(0); BARRIER(); STAMP(25); SBAR();
        rdWo(wf, xslot(w4));
        asm volatile("s_waitcnt lgkmcnt(0)" ::: "memory");
#pragma unroll
        for (int j = 0; j < 8; ++j) lds_w128(scr + j * 1024, f[j]);
        *(LAS float*)(uintptr_t)(L0 + xslot(w4) + 8192 + lane * 4) = lseA;
#pragma unroll
        for (int j = 0; j < 4; ++j) { dmaO1(Wob + pcol(6) * 64, Lq + KR + 2 * TILEB, j); dmaO1(Wob + pcol(7) * 64, Lq + VR + 4 * TILEB, j); }
        STAMP(4);
        BARRIER();
        VMCNT(0);
        BARRIER();
        BARRIER();
    } else {
        const float lseB = m + __builtin_amdgcn_logf(l), invl = 1.f / l;
        const __amdgpu_buffer_rsrc_t p1o = __builtin_amdgcn_make_buffer_rsrc(ws + WS_P1 + (size_t)(me * 4 + w4) * 9216, 0, 9216, 0x00020000);
        const __amdgpu_buffer_rsrc_t p1i = __builtin_amdgcn_make_buffer_rsrc(ws + WS_P1 + (size_t)((me ^ 1) * 4 + w4) * 9216, 0, 9216, 0x00020000);
        STAMP(4);
        BARRIER();
        const float lseA = *(const LAS float*)(uintptr_t)(L0 + xslot(w4) + 8192 + lane * 4);
        u32x4 pa[8];
#pragma unroll
        for (int j = 0; j < 8; ++j) pa[j] = *(const LAS u32x4*)(uintptr_t)(scr + j * 1024);
        const float mx = __builtin_fmaxf(lseA, lseB), ea = __builtin_amdgcn_exp2f(lseA - mx), eb = __builtin_amdgcn_exp2f(lseB - mx);
        const float den = ea + eb, rden = __builtin_amdgcn_rcpf(den), ca = ea * rden, cb = eb * rden * invl;
        const float lse = mx + __builtin_amdgcn_logf(den);
        __builtin_amdgcn_raw_buffer_store_b32(__float_as_uint(lse), p1o, 8192 + lane * 4, 0, 16);
#pragma unroll
        for (int j = 0; j < 8; ++j) {
            u32x4 f;
#pragma unroll
            for (int e = 0; e < 4; ++e) {
                const int r = 8 * (j & 1) + 2 * e;
                o[j >> 1][r] = o[j >> 1][r] * cb + bflo(pa[j][e]) * ca; o[j >> 1][r + 1] = o[j >> 1][r + 1] * cb + bfhi(pa[j][e]) * ca;
                f[e] = cvtpk(o[j >> 1][r], o[j >> 1][r + 1]);
            }
            st_sc1(p1o, j * 1024 + lane * 16, f);
        }
        VMCNT(0);
        if (lane == 0) __hip_atomic_store((GAS u64*)(ws + WS_F1 + (me * 4 + w4) * 128), tag, RLX_AGENT);
        STAMP(5);
        BARRIER();
        rdWo(wf, (w4 & 1) ? VR + (3 + (w4 >> 1)) * TILEB : KR + w4 * TILEB);
        wait_flag((GAS u64*)(ws + WS_F1 + ((me ^ 1) * 4 + w4) * 128), tag);
        STAMP(26);
        const float lseR = __uint_as_float(__builtin_amdgcn_raw_buffer_load_b32(p1i, 8192 + lane * 4, 0, 16));
        u32x4 rp[8];
#pragma unroll
        for (int j = 0; j < 8; ++j) rp[j] = ld_sc1(p1i, j * 1024 + lane * 16);
        const float mx2 = __builtin_fmaxf(lse, lseR), e0 = __builtin_amdgcn_exp2f(lse - mx2), e1 = __builtin_amdgcn_exp2f(lseR - mx2);
        const float rd2 = __builtin_amdgcn_rcpf(e0 + e1), c0 = e0 * rd2, c1 = e1 * rd2;
#pragma unroll
        for (int j = 0; j < 8; ++j) {
            u32x4 f;
#pragma unroll
            for (int e = 0; e < 4; ++e) { const int r = 8 * (j & 1) + 2 * e; f[e] = cvtpk(o[j >> 1][r] * c0 + bflo(rp[j][e]) * c1, o[j >> 1][r + 1] * c0 + bfhi(rp[j][e]) * c1); }
            lds_w128(scr + j * 1024, f);
        }
        BARRIER();
    }
    STAMP(27);
    {
        const __amdgpu_buffer_rsrc_t p2 = __builtin_amdgcn_make_buffer_rsrc(ws + WS_P2 + (size_t)(b * 2 + sp) * P2STRIDE, 0, 16 * 36864, 0x00020000);
        auto put = [&](const f32x16& c, unsigned piece) {
#pragma unroll
            for (int u = 0; u < 2; ++u) {
                u32x4 f;
#pragma unroll
                for (int e = 0; e < 4; ++e) f[e] = cvtpk(c[8 * u + 2 * e], c[8 * u + 2 * e + 1]);
                st_sc1(p2, kvh * 36864 + (piece + u) * 1024 + lane * 16, f);
            }
        };
        constexpr int LB = 6;
        const unsigned rbf = wid & 1;
        const unsigned ob[4] = {L0 + xslot(rbf) + lane * 16, L0 + xslot(2 + rbf) + lane * 16, L0 + xslot(rbf ^ 1) + lane * 16, L0 + xslot(2 + (rbf ^ 1)) + lane * 16};
        {
            bf16x8 f[32]; f32x16 c = {}, c2 = {};
            auto rd = [&](int i) { f[i] = lds_b128(ob[i >> 3] + (i & 7) * 1024); };
#pragma unroll
            for (int i = 0; i < LB; ++i) rd(i);
            SBAR();
#pragma unroll
            for (int i = 0; i < 32; ++i) {
                if (i + LB < 32) rd(i + LB);
                if (i < 16) c = MFMA(wf[i], f[i], c); else c2 = MFMA(wf[i - 16], f[i], c2);
                if (i == 19) put(c, wid * 4 + rbf * 2);
                SBAR();
            }
            if (wid < 2) {
                bf16x8 fa[16]; c = f32x16{};
                auto rd8 = [&](int i) { const unsigned a = L0 + KR + 1 * TILEB + wor + i * 1024; fa[i] = cat(lds_tr(a), lds_tr(a + 512)); f[i] = lds_b128(ob[i >> 3] + (i & 7) * 1024); };
#pragma unroll
                for (int i = 0; i < 4; ++i) rd8(i);
                SBAR();
#pragma unroll
                for (int i = 0; i < 16; ++i) {
                    if (i + 4 < 16) rd8(i + 4);
                    c = MFMA(fa[i], f[i], c);
                    if (i == 3) put(c2, wid * 4 + (rbf ^ 1) * 2);
                    SBAR();
                }
                put(c, 32 + wid * 2);
            } else put(c2, wid * 4 + (rbf ^ 1) * 2);
        }
        VMCNT(0);
        GAS u64* f2 = (GAS u64*)(ws + WS_F2);
        {
            const unsigned cons = lane < 2 ? wid * 2 + lane : wid * 8 + (lane - 2);
            if (lane < (wid < 2 ? 10u : 2u)) __hip_atomic_store(f2 + (size_t)((b * HKV + cons) * 2 + sp) * 32 + (lane < 2 ? 0 : 16) + kvh, tag, RLX_AGENT);
        }
        STAMP(28);
        {
            if (wid == 7) wait_flag(f2 + (size_t)me * 32 + (lane & 31), tag);
            BAR0();
            STAMP(29);
            if (wid == 0 && kvh == 0 && sp == 0 && lane == 0) __hip_atomic_store((GAS u64*)(ws + WS_EPOCH + b * 256), tag, RLX_AGENT);
            const unsigned sg = lane >> 4, c16 = lane & 15;
            auto fin = [&](const u32x4* v, unsigned cc) {
                float acc[8];
#pragma unroll
                for (int e = 0; e < 4; ++e) { acc[2 * e] = (bflo(v[0][e]) + bflo(v[1][e])) + (bflo(v[2][e]) + bflo(v[3][e])); acc[2 * e + 1] = (bfhi(v[0][e]) + bfhi(v[1][e])) + (bfhi(v[2][e]) + bfhi(v[3][e])); }
#pragma unroll
                for (int e = 0; e < 8; ++e) {
                    acc[e] = swapadd(acc[e]);
                    auto r = __builtin_amdgcn_permlane16_swap(__float_as_uint(acc[e]), __float_as_uint(acc[e]), false, false);
                    acc[e] = __uint_as_float(r[0]) + __uint_as_float(r[1]);
                }
                const unsigned piece = cc >> 6, ln = cc & 63, nb = piece >> 2, rb = (piece >> 1) & 1, u = piece & 1;
                char* op = Out + ((size_t)(b * 64 + rb * 32 + (ln & 31)) * DM + sp * 288 + pcol(nb) * 32 + u * 16 + (ln >> 5) * 4) * 2;
                if (sg < 2) *(GAS u32x2*)(op + sg * 16) = sg ? u32x2{cvtpk(acc[4], acc[5]), cvtpk(acc[6], acc[7])} : u32x2{cvtpk(acc[0], acc[1]), cvtpk(acc[2], acc[3])};
            };
            const unsigned cc = kvh * 128 + wid * 16 + c16, c8 = 2048 + kvh * 16 + c16;
            u32x4 v[4], v8[4];
#pragma unroll
            for (int s = 0; s < 4; ++s) v[s] = ld_sc1(p2, (sg * 4 + s) * 36864 + cc * 16);
            if (wid == 7) {
#pragma unroll
                for (int s = 0; s < 4; ++s) v8[s] = ld_sc1(p2, (sg * 4 + s) * 36864 + c8 * 16);
            }
            fin(v, cc);
            if (wid == 7) fin(v8, c8);
            STAMP(30);
        }
    }
}

extern "C" void kernel_launch(void* const* d_in, const int* in_sizes, int n_in, void* d_out, int out_size, void* d_ws, size_t ws_size, hipStream_t stream) {
    (void)hipFuncSetAttribute((const void*)gqa_fused, hipFuncAttributeMaxDynamicSharedMemorySize, LDS_BYTES);
    hipLaunchKernelGGL(gqa_fused, dim3(256), dim3(512), LDS_BYTES, stream, (const char*)d_in[0], (const char*)d_in[1], (const char*)d_in[2], (const char*)d_in[3],
                       (const char*)d_in[4], (char*)d_out, (char*)d_ws + WS_BASE);
}
```

```cpp
#include <hip/hip_runtime.h>
#include <stdint.h>
#include <type_traits>

#define DI __device__ __forceinline__
#define LAS __attribute__((address_space(3)))
#define GAS __attribute__((address_space(1)))
typedef __bf16 bf16x8 __attribute__((ext_vector_type(8)));
typedef __bf16 bf16x2 __attribute__((ext_vector_type(2)));
typedef short s16x4 __attribute__((ext_vector_type(4)));
typedef short s16x8 __attribute__((ext_vector_type(8)));
typedef float f32x2 __attribute__((ext_vector_type(2)));
typedef float f32x16 __attribute__((ext_vector_type(16)));
typedef unsigned u32x4 __attribute__((ext_vector_type(4)));
typedef unsigned u32x2 __attribute__((ext_vector_type(2)));
typedef unsigned long long u64;

constexpr int SQ = 64, DM = 576, HKV = 16, SK = 2048;
constexpr unsigned TILEB = 16384;
constexpr unsigned KR = 0, VR = 5 * TILEB;
constexpr unsigned SCR = 8 * TILEB;
constexpr unsigned LDS_BYTES = 10 * TILEB;
constexpr unsigned CODE_BYTES = 24000;
constexpr float QSCALE = 0.08838834764831845f * 1.4426950408889634f;

#define AI __attribute__((always_inline))
#define SBAR() __builtin_amdgcn_sched_barrier(0)
#define MFMA(a, b, c) __builtin_amdgcn_mfma_f32_32x32x16_bf16((a), (b), (c), 0, 0, 0)
#define VMCNT(n) asm volatile("s_waitcnt vmcnt(%0)" :: "i"(n) : "memory")
#define BARRIER() asm volatile("s_waitcnt lgkmcnt(0)\n\ts_barrier" ::: "memory")
#define BAR0() asm volatile("s_barrier" ::: "memory")
#define RLX_AGENT __ATOMIC_RELAXED, __HIP_MEMORY_SCOPE_AGENT

DI unsigned cvtpk(float lo, float hi) { f32x2 v = {lo, hi}; bf16x2 b = __builtin_convertvector(v, bf16x2); return __builtin_bit_cast(unsigned, b); }
DI float bflo(unsigned u) { return __uint_as_float(u << 16); }
DI float bfhi(unsigned u) { return __uint_as_float(u & 0xffff0000u); }
DI bf16x8 lds_b128(unsigned a) { return *(const LAS bf16x8*)(uintptr_t)a; }
DI s16x4 lds_tr(unsigned a) { return __builtin_amdgcn_ds_read_tr16_b64_v4i16((LAS s16x4*)(uintptr_t)a); }
DI bf16x8 cat(s16x4 lo, s16x4 hi) { s16x8 r = __builtin_shufflevector(lo, hi, 0, 1, 2, 3, 4, 5, 6, 7); return __builtin_bit_cast(bf16x8, r); }
DI void lds_w128(unsigned a, u32x4 v) { *(LAS u32x4*)(uintptr_t)a = v; }
template <int NT, int OFF> DI void dma1(const void* sbase, unsigned voff, unsigned m) {
    if constexpr (NT) asm volatile("s_mov_b32 m0, %2\n\ts_nop 0\n\tglobal_load_lds_dwordx4 %0, %1 offset:%3 nt" :: "v"(voff), "s"(sbase), "s"(m), "i"(OFF) : "memory");
    else asm volatile("s_mov_b32 m0, %2\n\ts_nop 0\n\tglobal_load_lds_dwordx4 %0, %1 offset:%3" :: "v"(voff), "s"(sbase), "s"(m), "i"(OFF) : "memory");
}
template <int NT, int OFF> DI void dma2(const void* sbase, unsigned v0, unsigned v1, unsigned m) {
    if constexpr (NT) asm volatile("s_mov_b32 m0, %3\n\ts_nop 0\n\tglobal_load_lds_dwordx4 %0, %2 offset:%4 nt\n\tglobal_load_lds_dwordx4 %1, %2 offset:%5 nt" :: "v"(v0), "v"(v1), "s"(sbase), "s"(m), "i"(OFF), "i"(OFF + 1024) : "memory");
    else asm volatile("s_mov_b32 m0, %3\n\ts_nop 0\n\tglobal_load_lds_dwordx4 %0, %2 offset:%4\n\tglobal_load_lds_dwordx4 %1, %2 offset:%5" :: "v"(v0), "v"(v1), "s"(sbase), "s"(m), "i"(OFF), "i"(OFF + 1024) : "memory");
}
DI f32x16 mfma_c(bf16x8 a, bf16x8 b, const f32x16& c) {
    f32x16 d; asm("s_nop 1\n\tv_mfma_f32_32x32x16_bf16 %0, %1, %2, %3" : "=&v"(d) : "v"(a), "v"(b), "v"(c)); return d; }
DI float fsub_s(float a, float b) { float r; asm("v_sub_f32_e32 %0, %1, %2" : "=v"(r) : "v"(a), "v"(b)); return r; }
DI float swapmax(float x) { auto r = __builtin_amdgcn_permlane32_swap(__float_as_uint(x), __float_as_uint(x), false, false); return __builtin_fmaxf(__uint_as_float(r[0]), __uint_as_float(r[1])); }
DI float swapadd(float x) { auto r = __builtin_amdgcn_permlane32_swap(__float_as_uint(x), __float_as_uint(x), false, false); return __uint_as_float(r[0]) + __uint_as_float(r[1]); }
#define MX3(a, b, c) __builtin_fmaxf(__builtin_fmaxf((a), (b)), (c))
DI void st_sc1(__amdgpu_buffer_rsrc_t r, unsigned off, u32x4 v) { __builtin_amdgcn_raw_buffer_store_b128(v, r, off, 0, 16); }
DI u32x4 ld_sc1(__amdgpu_buffer_rsrc_t r, unsigned off) { return __builtin_amdgcn_raw_buffer_load_b128(r, off, 0, 16); }
DI void wait_flag(GAS u64* f, u64 tag) {
    u64 v0 = __hip_atomic_load(f, RLX_AGENT);
    if (!__all(v0 == tag)) {
        v0 = __hip_atomic_load(f, RLX_AGENT);
        __builtin_amdgcn_s_sleep(8);
#pragma unroll 1
        for (unsigned spin = 0; spin < (1u << 20); ++spin) {
            const u64 v1 = __hip_atomic_load(f, RLX_AGENT);
            if (__all(v0 == tag)) break;
            v0 = __hip_atomic_load(f, RLX_AGENT);
            if (__all(v1 == tag)) break;
        }
    }
    asm volatile("" ::: "memory");
}

constexpr size_t WS_EPOCH = 0;
constexpr size_t WS_F1 = 65536;
constexpr size_t WS_F2 = 262144;
constexpr size_t WS_P1 = 1u << 20;
constexpr size_t WS_P2 = 12u << 20;
constexpr unsigned P2STRIDE = 16 * 36864;

using T_ = std::integral_constant<bool, true>; using F_ = std::integral_constant<bool, false>;

__global__ __launch_bounds__(512, 2) void gqa_fused(const char* X, const char* Kc, const char* Vc, const char* Wq, const char* Wo, char* Out, char* ws) {
    extern __shared__ __attribute__((aligned(16))) char smem[];
    asm volatile("" : "+s"(X), "+s"(Kc), "+s"(Vc), "+s"(Wq), "+s"(Wo), "+s"(Out), "+s"(ws));
    u64 pc0; asm volatile("s_getpc_b64 %0" : "=s"(pc0));
    const unsigned tid = threadIdx.x, lane = tid & 63;
    const unsigned wid = __builtin_amdgcn_readfirstlane(tid >> 6);
    const unsigned r32 = lane & 31, hi = lane >> 5, w4 = wid & 3, g = wid >> 2;
    const unsigned L0 = (unsigned)(uintptr_t)smem;
    const unsigned wg = blockIdx.x, xcd = wg & 7, slot = wg >> 3;
    const unsigned kvh = (xcd & 3) * 4 + (slot & 3), sp = xcd >> 2, b = slot >> 2;
    const unsigned pair = b * HKV + kvh, me = pair * 2 + sp;
    constexpr unsigned TS = 16384;
    const unsigned woff = sp * 262144 + w4 * 4096;
    const char* Kb = Kc + (size_t)pair * SK * 256 + woff;
    const char* Vb = Vc + (size_t)pair * SK * 256 + woff;
    const unsigned kv0 = (lane >> 4) * 256 + (((lane & 15) ^ (lane >> 4)) << 4), kv1 = kv0 ^ 64, kv2 = kv0 ^ 128, kv3 = kv0 ^ 192;
    const unsigned vvoff = (lane >> 4) * 256 + (((lane & 15) ^ ((lane >> 4) << 2)) << 4);
    const unsigned Lw = L0 + w4 * 1024, Lq = L0 + w4 * 4096;
    const unsigned rot = b * 2;
    auto dmaK = [&](unsigned t, unsigned sl) { t = (t + rot) & 15; dma2<1, 0>(Kb + t * TS, kv0, kv1, Lq + KR + sl * TILEB); dma2<1, 2048>(Kb + t * TS, kv2, kv3, Lq + KR + sl * TILEB); };
    auto dmaV = [&](unsigned t, unsigned sl) { t = (t + rot) & 15; dma2<1, 0>(Vb + t * TS, vvoff, vvoff, Lq + VR + sl * TILEB); dma2<1, 2048>(Vb + t * TS, vvoff, vvoff, Lq + VR + sl * TILEB); };
    const unsigned g1 = (lane >> 4) & 1, q4 = (lane & 15) >> 2, p4 = lane & 3;

    bf16x8 qf[8];
    auto touch = [&](const void* sbase, unsigned voff, unsigned dst) {
        asm volatile("s_mov_b32 m0, %2\n\ts_nop 0\n\tglobal_load_lds_dword %0, %1" :: "v"(voff), "s"(sbase), "s"(dst) : "memory");
    };
    {
        constexpr unsigned XR = 3 * TILEB, WR = 6 * TILEB;
        if (g) {
            const char* Xb = X + (size_t)b * SQ * DM * 2;
            const unsigned xvoff = (8 * w4 + (lane >> 3)) * (DM * 2) + (((lane & 7) ^ (4 * (w4 & 1) + (lane >> 4))) << 4);
            const char* Wb = Wq + (2 * kvh + (w4 >> 1)) * 256 + (size_t)(w4 & 1) * 16 * 8192;
            const unsigned wvoff = (lane >> 4) * 8192 + (((lane & 15) ^ ((lane >> 4) << 2)) << 4);
            unsigned cb = b + (kvh & 3) * 2; cb = cb >= 9 ? cb - 9 : cb;
            unsigned xb = cb;
            auto xhalf = [&](unsigned j, unsigned h) { dma1<0, 0>(Xb + xb * 128 + h * (32 * DM * 2), xvoff, Lw + XR + (j & 3) * 8192 + h * 4096); if (h) xb = xb == 8 ? 0 : xb + 1; };
            auto phase = [&](unsigned p) {
                xhalf((p + 1) >> 1, (p & 1) ^ 1);
                const char* src = Wb + (size_t)(cb * 2 + (p & 1)) * (32 * 8192);
                dma2<0, 0>(src, wvoff, wvoff + 31744, Lq + WR + (p & 3) * TILEB); dma2<0, 2048>(src, wvoff + 2 * 31744, wvoff + 3 * 31744, Lq + WR + (p & 3) * TILEB);
                if (p & 1) cb = cb == 8 ? 0 : cb + 1;
            };
            for (unsigned i = 0; i < (b & 3) * 4; ++i) __builtin_amdgcn_s_sleep(4);
            xhalf(0, 0);
            phase(0); phase(1); phase(2);
            VMCNT(10); BAR0();
            phase(3);
#pragma unroll 1
            for (unsigned p = 4; p < 18; ++p) { VMCNT(10); BAR0(); phase(p); }
            VMCNT(10); BAR0(); dmaK(0, 0);
            VMCNT(9); BAR0(); dmaK(1, 1);
            VMCNT(8); BAR0(); dmaK(2, 2);
            BAR0();
            dmaV(0, 0);
            VMCNT(12); BAR0();
#pragma unroll
            for (int s = 0; s < 8; ++s) qf[s] = lds_b128(L0 + SCR + (w4 * 8 + s) * 1024 + lane * 16);
            SBAR();
            dmaK(3, 3); dmaV(1, 1);
            VMCNT(16);
        } else {
            if (w4 == 0) { const unsigned ln = slot * 8 + (lane & 7); touch((const void*)(pc0 & ~127ull), ln * 128 < CODE_BYTES ? ln * 128 : 0u, Lw + VR); }
            const unsigned ps = ((p4 & 1) << 1) | (p4 >> 1);
            const unsigned wa0 = (L0 + WR + (w4 >> 1) * 8192 + (8 * hi + q4) * 256 + (q4 << 6) + g1 * 32 + ps * 8) ^ ((w4 & 1) << 7);
            const unsigned xa0 = L0 + XR + r32 * 128 + ((hi ^ ((r32 >> 1) & 7)) << 4);
            f32x16 acc[4] = {};
            bf16x8 fa[2][4], fb[2][4];
            auto rdA = [&](int set, unsigned p, int i) { const unsigned a = ((wa0 + (p & 3) * TILEB) ^ ((i & 1) << 6)) + (i >> 1) * 4096; fa[set][i] = cat(lds_tr(a), lds_tr(a + 1024)); };
            auto rdB = [&](int set, unsigned p, int i) { fb[set][i] = lds_b128(((xa0 + ((p >> 1) & 3) * 8192) ^ ((2 * set + (i >> 1)) << 5)) + (i & 1) * 4096); };
            auto step = [&](auto set_, auto last_, unsigned pn) AI {
                constexpr int set = decltype(set_)::value; constexpr bool last = decltype(last_)::value;
                SBAR(); BARRIER(); SBAR();
                __builtin_amdgcn_s_sleep(2);
#pragma unroll
                for (int i = 0; i < 8; ++i) {
                    acc[i & 3] = MFMA(fa[set][2 * (i >> 2) + ((i >> 1) & 1)], fb[set][2 * (i >> 2) + (i & 1)], acc[i & 3]);
                    if (!last && i < 4) { rdA(set ^ 1, pn, i); rdB(set ^ 1, pn, i); }
                    SBAR();
                }
            };
            using I0 = std::integral_constant<int, 0>; using I1 = std::integral_constant<int, 1>;
            BAR0();
#pragma unroll
            for (int i = 0; i < 4; ++i) { rdA(0, 0, i); rdB(0, 0, i); }
#pragma unroll 1
            for (unsigned p = 0; p < 16; p += 2) { if (p == 12) VMCNT(0); step(I0{}, F_{}, p + 1); step(I1{}, F_{}, p + 2); }
            step(I0{}, F_{}, 17); step(I1{}, T_{}, 0);
#pragma unroll
            for (int s = 0; s < 8; ++s) {
                u32x4 f;
#pragma unroll
                for (int e = 0; e < 4; ++e) f[e] = cvtpk(acc[s >> 1][8 * (s & 1) + 2 * e] * QSCALE, acc[s >> 1][8 * (s & 1) + 2 * e + 1] * QSCALE);
                lds_w128(L0 + SCR + (((w4 & 2) + ((s >> 1) & 1)) * 8 + (w4 & 1) * 4 + (s >> 2) * 2 + (s & 1)) * 1024 + lane * 16, f);
            }
            BARRIER();
#pragma unroll
            for (int s = 0; s < 8; ++s) qf[s] = lds_b128(L0 + SCR + (w4 * 8 + s) * 1024 + lane * 16);
        }
    }

    const unsigned kr0 = L0 + KR + r32 * 256 + ((hi ^ (r32 & 15)) << 4);
    const unsigned vr0 = L0 + VR + (4 * hi + q4) * 256 + (q4 << 6) + g1 * 32 + p4 * 8;
    const char* Wob = Wo + ((size_t)(kvh * 256 + w4 * 64) * DM + sp * 288) * 2;
    const unsigned ov0 = (lane >> 2) * (DM * 2) + (lane & 3) * 16;
    constexpr unsigned OVS = 16 * DM * 2 - 1024;
    auto pcol = [&](unsigned nb) { return sp ? (nb == 8 ? 0u : nb + 1) : nb; };
    auto dmaO1 = [&](const char* src, unsigned m, int j) {
        if (j == 0) dma1<0, 0>(src, ov0, m); else if (j == 1) dma1<0, 1024>(src, ov0 + OVS, m); else if (j == 2) dma1<0, 2048>(src, ov0 + 2 * OVS, m); else dma1<0, 3072>(src, ov0 + 3 * OVS, m);
    };
    f32x16 o[4] = {};
    f32x16 s0, s1, negm = {};
    u32x4 pf[4];
    float m = 0.f, l = 0.f;

    constexpr int LA = 6;
    auto rdK = [&](unsigned ka, int i) { return lds_b128((ka ^ ((i >> 1) << 5)) + (i & 1) * 8192); };
    auto rdV = [&](unsigned va, int i) { const unsigned a = (va ^ ((i & 3) << 6)) + (i >> 2) * 4096; return cat(lds_tr(a), lds_tr(a + 2048)); };
    auto MPH = [&](auto doqk_, auto dopv_, unsigned ksl_, unsigned vsl_) AI {
        constexpr bool DQ = decltype(doqk_)::value, DP = decltype(dopv_)::value;
        constexpr int N = 16 * (int)DQ + 16 * (int)DP, OFFV = DQ ? 16 : 0;
        const unsigned ka = kr0 + ksl_ * TILEB, va = vr0 + vsl_ * TILEB;
        bf16x8 f[N];
        auto rd = [&](int i) { if (DQ && i < 16) f[i] = rdK(ka, i); else f[i] = rdV(va, i - OFFV); };
        __builtin_amdgcn_s_setprio(1);
#pragma unroll
        for (int i = 0; i < LA; ++i) rd(i);
        SBAR();
#pragma unroll
        for (int i = 0; i < N; ++i) {
            if (i + LA < N) rd(i + LA);
            if (DQ && i < 16) {
                if (i == 0) s0 = mfma_c(f[i], qf[0], negm); else if (i == 1) s1 = mfma_c(f[i], qf[0], negm);
                else if (i & 1) s1 = MFMA(f[i], qf[i >> 1], s1); else s0 = MFMA(f[i], qf[i >> 1], s0);
            } else { const int j = i - OFFV; o[j & 3] = MFMA(f[i], __builtin_bit_cast(bf16x8, pf[j >> 2]), o[j & 3]); }
            SBAR();
        }
        __builtin_amdgcn_s_setprio(0);
    };
    auto RESCALE = [&](auto first_) AI {
        constexpr bool first = decltype(first_)::value;
        float a = MX3(s0[0], s0[1], s1[0]), c = MX3(s0[2], s0[3], s1[1]); a = MX3(a, s1[2], s1[3]);
#pragma unroll
        for (int r = 4; r < 16; r += 4) { a = MX3(a, s0[r], s0[r + 1]); c = MX3(c, s0[r + 2], s0[r + 3]); a = MX3(a, s1[r], s1[r + 1]); c = MX3(c, s1[r + 2], s1[r + 3]); }
        const float rm = swapmax(__builtin_fmaxf(a, c));
        const float dl = first ? rm : __builtin_fmaxf(rm, 0.f);
        m += dl;
#pragma unroll
        for (int r = 0; r < 16; ++r) { s0[r] = fsub_s(s0[r], dl); s1[r] = fsub_s(s1[r], dl); negm[r] = fsub_s(negm[r], dl); }
        if (!first) {
            const float f = __builtin_amdgcn_exp2f(-dl);
            l *= f;
#pragma unroll
            for (int db = 0; db < 4; ++db) o[db] *= f;
        }
    };
    auto EXPS = [&](auto&& hook) AI {
        float sa = 0.f, sb = 0.f;
#pragma unroll
        for (int k = 0; k < 8; ++k) {
            const f32x16& s = k < 4 ? s0 : s1; const int r0 = (k & 3) * 4;
            const float e0 = __builtin_amdgcn_exp2f(s[r0]), e1 = __builtin_amdgcn_exp2f(s[r0 + 1]), e2 = __builtin_amdgcn_exp2f(s[r0 + 2]), e3 = __builtin_amdgcn_exp2f(s[r0 + 3]);
            if (k == 0) { sa = e0 + e2; sb = e1 + e3; } else { sa += e0; sb += e1; sa += e2; sb += e3; }
            unsigned w0 = cvtpk(e0, e1), w1 = cvtpk(e2, e3);
            asm volatile("" : "+v"(w0), "+v"(w1), "+v"(sa), "+v"(sb));
            pf[k >> 1][(k & 1) * 2] = w0; pf[k >> 1][(k & 1) * 2 + 1] = w1;
            SBAR(); hook(k); SBAR();
        }
        return sa + sb;
    };
    auto SM = [&](auto first_, auto&& hook) AI {
        constexpr bool first = decltype(first_)::value;
        if (first) RESCALE(first_);
        float ts = EXPS(hook);
        if (!first && __builtin_expect(__any(!(ts <= 256.f)), 0)) { RESCALE(F_{}); ts = EXPS([](int) {}); }
        l += ts;
        asm volatile("" : "+v"(l));
    };
    auto inc2 = [](unsigned s) { return s >= 3 ? s - 3 : s + 2; };
    unsigned t = g;
    unsigned kq = g, vq = g;
    const char* ksrc; const char* vsrc;
    unsigned mK, mV;
    auto hookKV = [&](int j) {
        if (j == 0) dma2<1, 0>(ksrc, kv0, kv1, mK); else if (j == 2) dma2<1, 2048>(ksrc, kv2, kv3, mK);
        else if (j == 4) dma2<1, 0>(vsrc, vvoff, vvoff, mV); else if (j == 6) dma2<1, 2048>(vsrc, vvoff, vvoff, mV);
    };
    auto prep = [&]() { ksrc = Kb + ((t + 4 + rot) & 15) * TS; vsrc = Vb + ((t + 2 + rot) & 15) * TS; mK = Lq + KR + (kq ? kq - 1 : 4) * TILEB; mV = Lq + VR + inc2(vq) * TILEB; asm volatile("" : "+s"(mK), "+s"(mV), "+s"(ksrc), "+s"(vsrc)); };
    if (g) { SBAR(); BARRIER(); SBAR(); }
    prep();
    MPH(T_{}, F_{}, kq, 0);
    SBAR();
    if (g) VMCNT(8);
    BARRIER(); SBAR();
    SM(T_{}, hookKV); SBAR();
    VMCNT(8);
#pragma unroll 1
    while (t <= 9) {
        const unsigned vcur = vq;
        kq = inc2(kq); vq = inc2(vq); t += 2; prep();
        SBAR(); BARRIER(); SBAR();
        MPH(T_{}, T_{}, kq, vcur);
        SBAR(); BARRIER(); SBAR();
        SM(F_{}, hookKV); SBAR();
        VMCNT(8);
    }
#pragma unroll 1
    while (t <= 13) {
        const unsigned vcur = vq;
        kq = inc2(kq); vq = inc2(vq); t += 2; prep();
        const char* oA = Wob + pcol(t == 12 ? 8 : 2 * t - 26) * 64; const char* oB = Wob + pcol(2 * t - 25) * 64;
        unsigned mA = Lq + KR + (t == 12 ? 1 : kq) * TILEB, mB = Lq + VR + (t - 12) * TILEB;
        asm volatile("" : "+s"(oA), "+s"(oB), "+s"(mA), "+s"(mB));
        SBAR(); BARRIER(); SBAR();
        MPH(T_{}, T_{}, kq, vcur);
        SBAR(); BARRIER(); SBAR();
        if (t <= 13) { hookKV(4); hookKV(6); }
        SBAR();
        SM(F_{}, [&](int j) { if (!(j & 1)) { dmaO1(oA, mA, j >> 1); if (t >= 13) dmaO1(oB, mB, j >> 1); } }); SBAR();
        if (t == 13 || t == 14) VMCNT(12); else VMCNT(8);
    }
    SBAR(); BARRIER(); SBAR();
    MPH(F_{}, T_{}, 0, vq);
    l = swapadd(l);

    const unsigned wor = (4 * hi + q4) * 64 + g1 * 32 + p4 * 8;
    bf16x8 wf[16];
    auto xslot = [](unsigned w) { return (w & 1) ? VR + (1 + (w >> 1)) * TILEB : KR + (3 + (w >> 1)) * TILEB; };
    u64 tag;
    auto TAIL = [&](auto G_) AI {
        constexpr int G = decltype(G_)::value;
        const __amdgpu_buffer_rsrc_t p1o = __builtin_amdgcn_make_buffer_rsrc(ws + WS_P1 + (size_t)(me * 8 + wid) * 4608, 0, 4608, 0x00020000);
        const __amdgpu_buffer_rsrc_t p1i = __builtin_amdgcn_make_buffer_rsrc(ws + WS_P1 + (size_t)((me ^ 1) * 8 + wid) * 4608, 0, 4608, 0x00020000);
        const unsigned xa = L0 + xslot(w4) + lane * 16, xb = L0 + ((w4 & 2) ? VR + 4 * TILEB : KR + 2 * TILEB) + (w4 & 1) * 8192 + lane * 16;
        const float inv = __builtin_amdgcn_rcpf(l);
        float lse = m + __builtin_amdgcn_logf(l);
        u32x4 f[4];
#pragma unroll
        for (int j = 0; j < 4; ++j) {
#pragma unroll
            for (int e = 0; e < 4; ++e) { const int r = 8 * (j & 1) + 2 * e; const f32x16& oo = o[2 * (G ^ 1) + (j >> 1)]; f[j][e] = cvtpk(oo[r] * inv, oo[r + 1] * inv); }
            asm volatile("" : "+v"(f[j]));
        }
        if (!G) {
            asm volatile("" : "+v"(lse));
            SBAR(); VMCNT(0);
            BARRIER(); SBAR();
#pragma unroll
            for (int ks = 0; ks < 16; ++ks) wf[ks] = cat(lds_tr(L0 + xslot(w4) + wor + ks * 1024), lds_tr(L0 + xslot(w4) + wor + ks * 1024 + 512));
            asm volatile("s_waitcnt lgkmcnt(0)" ::: "memory");
        }
        const unsigned xo = G ? xb : xa, xi = G ? xa : xb;
#pragma unroll
        for (int j = 0; j < 4; ++j) lds_w128(xo + j * 1024, f[j]);
        *(LAS float*)(uintptr_t)(xo - lane * 12 + 4096) = lse;
        if (!G) {
#pragma unroll
            for (int j = 0; j < 4; ++j) { dmaO1(Wob + pcol(6) * 64, L0 + xslot(w4) + 8192, j); dmaO1(Wob + pcol(7) * 64, L0 + xslot(w4) + 12288, j); }
        }
        BARRIER();
        const u64 epoch = __hip_atomic_load((GAS u64*)(ws + WS_EPOCH + b * 256), RLX_AGENT);
        const float lseO = *(const LAS float*)(uintptr_t)(xi - lane * 12 + 4096);
#pragma unroll
        for (int j = 0; j < 4; ++j) f[j] = *(const LAS u32x4*)(uintptr_t)(xi + j * 1024);
        const float mx = __builtin_fmaxf(lse, lseO), ea = __builtin_amdgcn_exp2f(lse - mx), eb = __builtin_amdgcn_exp2f(lseO - mx);
        const float rden = __builtin_amdgcn_rcpf(ea + eb), ca = ea * rden * inv, cb = eb * rden, lseL = mx + __builtin_amdgcn_logf(ea + eb);
        __builtin_amdgcn_raw_buffer_store_b32(__float_as_uint(lseL), p1o, 4096 + lane * 4, 0, 16);
#pragma unroll
        for (int j = 0; j < 4; ++j) {
            u32x4 w;
#pragma unroll
            for (int e = 0; e < 4; ++e) {
                const int r = 8 * (j & 1) + 2 * e; f32x16& oo = o[2 * G + (j >> 1)];
                oo[r] = oo[r] * ca + bflo(f[j][e]) * cb; oo[r + 1] = oo[r + 1] * ca + bfhi(f[j][e]) * cb;
                w[e] = cvtpk(oo[r], oo[r + 1]);
            }
            st_sc1(p1o, j * 1024 + lane * 16, w);
        }
        SBAR(); VMCNT(0); SBAR();
        tag = ((u64)__builtin_amdgcn_readfirstlane((unsigned)(epoch >> 32)) << 32 | (unsigned)__builtin_amdgcn_readfirstlane((unsigned)epoch)) * 0x9E3779B97F4A7C15ull + 0x632BE59BD9B4E019ull;
        if (lane == 0) __hip_atomic_store((GAS u64*)(ws + WS_F1 + (me * 8 + wid) * 64), tag, RLX_AGENT);
        BARRIER();
        if (G) {
#pragma unroll
            for (int ks = 0; ks < 16; ++ks) {
                const unsigned a = L0 + (w4 < 2 ? (w4 ? VR + 3 * TILEB : KR) + ks * 1024 : xslot(ks >> 2) + w4 * 4096 + (ks & 3) * 1024) + wor;
                wf[ks] = cat(lds_tr(a), lds_tr(a + 512));
            }
        }
        wait_flag((GAS u64*)(ws + WS_F1 + ((me ^ 1) * 8 + wid) * 64), tag);
        const float lseR = __uint_as_float(__builtin_amdgcn_raw_buffer_load_b32(p1i, 4096 + lane * 4, 0, 16));
#pragma unroll
        for (int j = 0; j < 4; ++j) f[j] = ld_sc1(p1i, j * 1024 + lane * 16);
        const float mx2 = __builtin_fmaxf(lseL, lseR), e0 = __builtin_amdgcn_exp2f(lseL - mx2), e1 = __builtin_amdgcn_exp2f(lseR - mx2);
        const float rd2 = __builtin_amdgcn_rcpf(e0 + e1), c0 = e0 * rd2, c1 = e1 * rd2;
#pragma unroll
        for (int j = 0; j < 4; ++j) {
            u32x4 w;
#pragma unroll
            for (int e = 0; e < 4; ++e) {
                const int r = 8 * (j & 1) + 2 * e; const f32x16& oo = o[2 * G + (j >> 1)];
                w[e] = cvtpk(oo[r] * c0 + bflo(f[j][e]) * c1, oo[r + 1] * c0 + bfhi(f[j][e]) * c1);
            }
            lds_w128(xa + (4 * G + j) * 1024, w);
        }
        BARRIER();
    };
    if (g) TAIL(std::integral_constant<int, 1>{}); else TAIL(std::integral_constant<int, 0>{});
    {
        const __amdgpu_buffer_rsrc_t p2 = __builtin_amdgcn_make_buffer_rsrc(ws + WS_P2 + (size_t)(b * 2 + sp) * P2STRIDE, 0, 16 * 36864, 0x00020000);
        auto put = [&](const f32x16& c, unsigned piece) {
#pragma unroll
            for (int u = 0; u < 2; ++u) {
                u32x4 f;
#pragma unroll
                for (int e = 0; e < 4; ++e) f[e] = cvtpk(c[8 * u + 2 * e], c[8 * u + 2 * e + 1]);
                st_sc1(p2, kvh * 36864 + (piece + u) * 1024 + lane * 16, f);
            }
        };
        constexpr int LB = 6;
        const unsigned rbf = wid & 1;
        const unsigned ob[4] = {L0 + xslot(rbf) + lane * 16, L0 + xslot(2 + rbf) + lane * 16, L0 + xslot(rbf ^ 1) + lane * 16, L0 + xslot(2 + (rbf ^ 1)) + lane * 16};
        {
            bf16x8 f[32]; f32x16 c = {}, c2 = {};
            auto rd = [&](int i) { f[i] = lds_b128(ob[i >> 3] + (i & 7) * 1024); };
#pragma unroll
            for (int i = 0; i < LB; ++i) rd(i);
            SBAR();
#pragma unroll
            for (int i = 0; i < 32; ++i) {
                if (i + LB < 32) rd(i + LB);
                if (i < 16) c = MFMA(wf[i], f[i], c); else c2 = MFMA(wf[i - 16], f[i], c2);
                if (i == 19) put(c, wid * 4 + rbf * 2);
                SBAR();
            }
            if (wid < 2) {
                bf16x8 fa[16]; c = f32x16{};
                auto rd8 = [&](int i) { const unsigned a = L0 + KR + 1 * TILEB + wor + i * 1024; fa[i] = cat(lds_tr(a), lds_tr(a + 512)); f[i] = lds_b128(ob[i >> 3] + (i & 7) * 1024); };
#pragma unroll
                for (int i = 0; i < 4; ++i) rd8(i);
                SBAR();
#pragma unroll
                for (int i = 0; i < 16; ++i) {
                    if (i + 4 < 16) rd8(i + 4);
                    c = MFMA(fa[i], f[i], c);
                    if (i == 3) put(c2, wid * 4 + (rbf ^ 1) * 2);
                    SBAR();
                }
                put(c, 32 + wid * 2);
            } else put(c2, wid * 4 + (rbf ^ 1) * 2);
        }
        VMCNT(0);
        GAS u64* f2 = (GAS u64*)(ws + WS_F2);
        {
            const unsigned cons = lane < 2 ? wid * 2 + lane : wid * 8 + (lane - 2);
            if (lane < (wid < 2 ? 10u : 2u)) __hip_atomic_store(f2 + (size_t)((b * HKV + cons) * 2 + sp) * 32 + (lane < 2 ? 0 : 16) + kvh, tag, RLX_AGENT);
        }
        {
            if (wid == 7) wait_flag(f2 + (size_t)me * 32 + (lane & 31), tag);
            const unsigned sg = lane >> 4, c16 = lane & 15;
            const unsigned cc = kvh * 128 + wid * 16 + c16, c8 = 2048 + kvh * 16 + c16;
            auto oaddr = [&](unsigned c) {
                const unsigned piece = c >> 6, ln = c & 63, nb = piece >> 2, rb = (piece >> 1) & 1, u = piece & 1;
                return Out + ((size_t)(b * 64 + rb * 32 + (ln & 31)) * DM + sp * 288 + pcol(nb) * 32 + u * 16 + (ln >> 5) * 4 + (sg >> 1) * 8 + (sg & 1) * 2) * 2;
            };
            char* op = oaddr(cc); char* op8 = oaddr(c8);
            asm volatile("" : "+v"(op), "+v"(op8));
            BAR0();
            auto fin = [&](const u32x4* v, char* o_) {
                float a[8];
#pragma unroll
                for (int e = 0; e < 4; ++e) { a[2 * e] = (bflo(v[0][e]) + bflo(v[1][e])) + (bflo(v[2][e]) + bflo(v[3][e])); a[2 * e + 1] = (bfhi(v[0][e]) + bfhi(v[1][e])) + (bfhi(v[2][e]) + bfhi(v[3][e])); }
                float h[4], q[2];
#pragma unroll
                for (int e = 0; e < 4; ++e) { auto r = __builtin_amdgcn_permlane32_swap(__float_as_uint(a[e]), __float_as_uint(a[e + 4]), false, false); h[e] = __uint_as_float(r[0]) + __uint_as_float(r[1]); }
#pragma unroll
                for (int e = 0; e < 2; ++e) { auto r = __builtin_amdgcn_permlane16_swap(__float_as_uint(h[e]), __float_as_uint(h[e + 2]), false, false); q[e] = __uint_as_float(r[0]) + __uint_as_float(r[1]); }
                *(GAS unsigned*)o_ = cvtpk(q[0], q[1]);
            };
            u32x4 v[4], v8[4];
#pragma unroll
            for (int s = 0; s < 4; ++s) v[s] = ld_sc1(p2, (sg * 4 + s) * 36864 + cc * 16);
            if (wid == 6) {
#pragma unroll
                for (int s = 0; s < 4; ++s) v8[s] = ld_sc1(p2, (sg * 4 + s) * 36864 + c8 * 16);
            }
            if (wid == 0 && kvh == 0 && sp == 0 && lane == 0) __hip_atomic_store((GAS u64*)(ws + WS_EPOCH + b * 256), tag, RLX_AGENT);
            fin(v, op);
            if (wid == 6) fin(v8, op8);
        }
    }
}

extern "C" void kernel_launch(void* const* d_in, const int* in_sizes, int n_in, void* d_out, int out_size, void* d_ws, size_t ws_size, hipStream_t stream) {
    (void)hipFuncSetAttribute((const void*)gqa_fused, hipFuncAttributeMaxDynamicSharedMemorySize, LDS_BYTES);
    hipLaunchKernelGGL(gqa_fused, dim3(256), dim3(512), LDS_BYTES, stream, (const char*)d_in[0], (const char*)d_in[1], (const char*)d_in[2], (const char*)d_in[3],
                       (const char*)d_in[4], (char*)d_out, (char*)d_ws);
}
```

```cpp
#include <hip/hip_runtime.h>
#include <stdint.h>
#include <type_traits>

#define DI __device__ __forceinline__
#define LAS __attribute__((address_space(3)))
#define GAS __attribute__((address_space(1)))
typedef __bf16 bf16x8 __attribute__((ext_vector_type(8)));
typedef __bf16 bf16x2 __attribute__((ext_vector_type(2)));
typedef short s16x4 __attribute__((ext_vector_type(4)));
typedef short s16x8 __attribute__((ext_vector_type(8)));
typedef float f32x2 __attribute__((ext_vector_type(2)));
typedef float f32x16 __attribute__((ext_vector_type(16)));
typedef unsigned u32x4 __attribute__((ext_vector_type(4)));
typedef unsigned u32x2 __attribute__((ext_vector_type(2)));
typedef unsigned long long u64;

constexpr int SQ = 64, DM = 576, HKV = 16, SK = 2048;
constexpr unsigned TILEB = 16384;
constexpr unsigned KR = 0, VR = 5 * TILEB;
constexpr unsigned SCR = 8 * TILEB;
constexpr unsigned LDS_BYTES = 10 * TILEB;
constexpr unsigned CODE_BYTES = 25600;
constexpr float QSCALE = 0.08838834764831845f * 1.4426950408889634f;

#define AI __attribute__((always_inline))
#define SBAR() __builtin_amdgcn_sched_barrier(0)
#define MFMA(a, b, c) __builtin_amdgcn_mfma_f32_32x32x16_bf16((a), (b), (c), 0, 0, 0)
#define VMCNT(n) asm volatile("s_waitcnt vmcnt(%0)" :: "i"(n) : "memory")
#define BARRIER() asm volatile("s_waitcnt lgkmcnt(0)\n\ts_barrier" ::: "memory")
#define BAR0() asm volatile("s_barrier" ::: "memory")
#define RLX_AGENT __ATOMIC_RELAXED, __HIP_MEMORY_SCOPE_AGENT

DI unsigned cvtpk(float lo, float hi) { f32x2 v = {lo, hi}; bf16x2 b = __builtin_convertvector(v, bf16x2); return __builtin_bit_cast(unsigned, b); }
DI float bflo(unsigned u) { return __uint_as_float(u << 16); }
DI float bfhi(unsigned u) { return __uint_as_float(u & 0xffff0000u); }
DI bf16x8 lds_b128(unsigned a) { return *(const LAS bf16x8*)(uintptr_t)a; }
DI s16x4 lds_tr(unsigned a) { return __builtin_amdgcn_ds_read_tr16_b64_v4i16((LAS s16x4*)(uintptr_t)a); }
DI bf16x8 cat(s16x4 lo, s16x4 hi) { s16x8 r = __builtin_shufflevector(lo, hi, 0, 1, 2, 3, 4, 5, 6, 7); return __builtin_bit_cast(bf16x8, r); }
DI void lds_w128(unsigned a, u32x4 v) { *(LAS u32x4*)(uintptr_t)a = v; }
template <int NT, int OFF> DI void dma1(const void* sbase, unsigned voff, unsigned m) {
    if constexpr (NT) asm volatile("s_mov_b32 m0, %2\n\ts_nop 0\n\tglobal_load_lds_dwordx4 %0, %1 offset:%3 nt" :: "v"(voff), "s"(sbase), "s"(m), "i"(OFF) : "memory");
    else asm volatile("s_mov_b32 m0, %2\n\ts_nop 0\n\tglobal_load_lds_dwordx4 %0, %1 offset:%3" :: "v"(voff), "s"(sbase), "s"(m), "i"(OFF) : "memory");
}
template <int NT, int OFF> DI void dma2(const void* sbase, unsigned v0, unsigned v1, unsigned m) {
    if constexpr (NT) asm volatile("s_mov_b32 m0, %3\n\ts_nop 0\n\tglobal_load_lds_dwordx4 %0, %2 offset:%4 nt\n\tglobal_load_lds_dwordx4 %1, %2 offset:%5 nt" :: "v"(v0), "v"(v1), "s"(sbase), "s"(m), "i"(OFF), "i"(OFF + 1024) : "memory");
    else asm volatile("s_mov_b32 m0, %3\n\ts_nop 0\n\tglobal_load_lds_dwordx4 %0, %2 offset:%4\n\tglobal_load_lds_dwordx4 %1, %2 offset:%5" :: "v"(v0), "v"(v1), "s"(sbase), "s"(m), "i"(OFF), "i"(OFF + 1024) : "memory");
}
DI f32x16 mfma_c(bf16x8 a, bf16x8 b, const f32x16& c) {
    f32x16 d; asm("s_nop 1\n\tv_mfma_f32_32x32x16_bf16 %0, %1, %2, %3" : "=&v"(d) : "v"(a), "v"(b), "v"(c)); return d; }
DI float fsub_s(float a, float b) { float r; asm("v_sub_f32_e32 %0, %1, %2" : "=v"(r) : "v"(a), "v"(b)); return r; }
DI float swapmax(float x) { auto r = __builtin_amdgcn_permlane32_swap(__float_as_uint(x), __float_as_uint(x), false, false); return __builtin_fmaxf(__uint_as_float(r[0]), __uint_as_float(r[1])); }
DI float swapadd(float x) { auto r = __builtin_amdgcn_permlane32_swap(__float_as_uint(x), __float_as_uint(x), false, false); return __uint_as_float(r[0]) + __uint_as_float(r[1]); }
#define MX3(a, b, c) __builtin_fmaxf(__builtin_fmaxf((a), (b)), (c))
DI void st_sc1(__amdgpu_buffer_rsrc_t r, unsigned off, u32x4 v) { __builtin_amdgcn_raw_buffer_store_b128(v, r, off, 0, 16); }
DI u32x4 ld_sc1(__amdgpu_buffer_rsrc_t r, unsigned off) { return __builtin_amdgcn_raw_buffer_load_b128(r, off, 0, 16); }
DI void wait_flag(GAS u64* f, u64 tag) {
    u64 v0 = __hip_atomic_load(f, RLX_AGENT);
    if (!__all(v0 == tag)) {
        v0 = __hip_atomic_load(f, RLX_AGENT);
        __builtin_amdgcn_s_sleep(8);
#pragma unroll 1
        for (unsigned spin = 0; spin < (1u << 20); ++spin) {
            const u64 v1 = __hip_atomic_load(f, RLX_AGENT);
            if (__all(v0 == tag)) break;
            v0 = __hip_atomic_load(f, RLX_AGENT);
            if (__all(v1 == tag)) break;
        }
    }
    asm volatile("" ::: "memory");
}

DI unsigned pace(unsigned tn, unsigned per) {
    unsigned nx;
    asm volatile("1:\n\ts_memrealtime s[98:99]\n\ts_waitcnt lgkmcnt(0)\n\ts_sub_i32 s99, s98, %1\n\ts_cmp_lt_i32 s99, 0\n\ts_cbranch_scc1 1b\n\ts_add_i32 %0, s98, %2"
                 : "=s"(nx) : "s"(tn), "s"(per) : "memory", "scc", "s98", "s99");
    return nx;
}

constexpr size_t WS_EPOCH = 0;
constexpr size_t WS_F1 = 65536;
constexpr size_t WS_F2 = 262144;
constexpr size_t WS_P1 = 1u << 20;
constexpr size_t WS_P2 = 12u << 20;
constexpr unsigned P2STRIDE = 16 * 36864;

using T_ = std::integral_constant<bool, true>; using F_ = std::integral_constant<bool, false>;

__global__ __launch_bounds__(512, 2) void gqa_fused(const char* X, const char* Kc, const char* Vc, const char* Wq, const char* Wo, char* Out, char* ws) {
    extern __shared__ __attribute__((aligned(16))) char smem[];
    asm volatile("" : "+s"(X), "+s"(Kc), "+s"(Vc), "+s"(Wq), "+s"(Wo), "+s"(Out), "+s"(ws));
    u64 pc0; asm volatile("s_getpc_b64 %0" : "=s"(pc0));
    const unsigned tid = threadIdx.x, lane = tid & 63;
    const unsigned wid = __builtin_amdgcn_readfirstlane(tid >> 6);
    const unsigned r32 = lane & 31, hi = lane >> 5, w4 = wid & 3, g = wid >> 2;
    const unsigned L0 = (unsigned)(uintptr_t)smem;
    const unsigned wg = blockIdx.x, xcd = wg & 7, slot = wg >> 3;
    const unsigned kvh = (xcd & 3) * 4 + (slot & 3), sp = xcd >> 2, b = slot >> 2;
    const unsigned pair = b * HKV + kvh, me = pair * 2 + sp;
    constexpr unsigned TS = 16384;
    const unsigned woff = sp * 262144 + w4 * 4096;
    const char* Kb = Kc + (size_t)pair * SK * 256 + woff;
    const char* Vb = Vc + (size_t)pair * SK * 256 + woff;
    const unsigned kv0 = (lane >> 4) * 256 + (((lane & 15) ^ (lane >> 4)) << 4), kv1 = kv0 ^ 64, kv2 = kv0 ^ 128, kv3 = kv0 ^ 192;
    const unsigned vvoff = (lane >> 4) * 256 + (((lane & 15) ^ ((lane >> 4) << 2)) << 4);
    const unsigned Lw = L0 + w4 * 1024, Lq = L0 + w4 * 4096;
    const unsigned rot = b * 2;
    auto dmaK = [&](unsigned t, unsigned sl) { t = (t + rot) & 15; dma2<1, 0>(Kb + t * TS, kv0, kv1, Lq + KR + sl * TILEB); dma2<1, 2048>(Kb + t * TS, kv2, kv3, Lq + KR + sl * TILEB); };
    auto dmaV = [&](unsigned t, unsigned sl) { t = (t + rot) & 15; dma2<1, 0>(Vb + t * TS, vvoff, vvoff, Lq + VR + sl * TILEB); dma2<1, 2048>(Vb + t * TS, vvoff, vvoff, Lq + VR + sl * TILEB); };
    const unsigned g1 = (lane >> 4) & 1, q4 = (lane & 15) >> 2, p4 = lane & 3;

    bf16x8 qf[8];
    auto touch = [&](const void* sbase, unsigned voff, unsigned dst) {
        asm volatile("s_mov_b32 m0, %2\n\ts_nop 0\n\tglobal_load_lds_dword %0, %1" :: "v"(voff), "s"(sbase), "s"(dst) : "memory");
    };
    {
        constexpr unsigned XR = 3 * TILEB, WR = 6 * TILEB;
        if (g) {
            const char* Xb = X + (size_t)b * SQ * DM * 2;
            const unsigned xvoff = (8 * w4 + (lane >> 3)) * (DM * 2) + (((lane & 7) ^ (4 * (w4 & 1) + (lane >> 4))) << 4);
            const char* Wb = Wq + (2 * kvh + (w4 >> 1)) * 256 + (size_t)(w4 & 1) * 16 * 8192;
            const unsigned wvoff = (lane >> 4) * 8192 + (((lane & 15) ^ ((lane >> 4) << 2)) << 4);
            unsigned cb = b + (kvh & 3) * 2; cb = cb >= 9 ? cb - 9 : cb;
            unsigned xb = cb;
            auto xhalf = [&](unsigned j, unsigned h) { dma1<0, 0>(Xb + xb * 128 + h * (32 * DM * 2), xvoff, Lw + XR + (j & 3) * 8192 + h * 4096); if (h) xb = xb == 8 ? 0 : xb + 1; };
            auto phase = [&](unsigned p) {
                xhalf((p + 1) >> 1, (p & 1) ^ 1);
                const char* src = Wb + (size_t)(cb * 2 + (p & 1)) * (32 * 8192);
                dma2<0, 0>(src, wvoff, wvoff + 31744, Lq + WR + (p & 3) * TILEB); dma2<0, 2048>(src, wvoff + 2 * 31744, wvoff + 3 * 31744, Lq + WR + (p & 3) * TILEB);
                if (p & 1) cb = cb == 8 ? 0 : cb + 1;
            };
            for (unsigned i = 0; i < (b & 3) * 4; ++i) __builtin_amdgcn_s_sleep(4);
            xhalf(0, 0);
            phase(0); phase(1); phase(2);
            VMCNT(10); BAR0();
            phase(3);
#pragma unroll 1
            for (unsigned p = 4; p < 18; ++p) { VMCNT(10); BAR0(); phase(p); }
            VMCNT(10); BAR0(); dmaK(0, 0);
            VMCNT(9); BAR0(); dmaK(1, 1);
            VMCNT(8); BAR0(); dmaK(2, 2);
            BAR0();
            dmaV(0, 0);
            VMCNT(12); BAR0();
#pragma unroll
            for (int s = 0; s < 8; ++s) qf[s] = lds_b128(L0 + SCR + (w4 * 8 + s) * 1024 + lane * 16);
            SBAR();
            dmaK(3, 3); dmaV(1, 1);
            VMCNT(16);
        } else {
            if (w4 == 0) { const unsigned ln = slot * 8 + (lane & 7); touch((const void*)(pc0 & ~127ull), ln * 128 < CODE_BYTES ? ln * 128 : 0u, Lw + VR); }
            const unsigned ps = ((p4 & 1) << 1) | (p4 >> 1);
            const unsigned wa0 = (L0 + WR + (w4 >> 1) * 8192 + (8 * hi + q4) * 256 + (q4 << 6) + g1 * 32 + ps * 8) ^ ((w4 & 1) << 7);
            const unsigned xa0 = L0 + XR + r32 * 128 + ((hi ^ ((r32 >> 1) & 7)) << 4);
            f32x16 acc[4] = {};
            bf16x8 fa[2][4], fb[2][4];
            auto rdA = [&](int set, unsigned p, int i) { const unsigned a = ((wa0 + (p & 3) * TILEB) ^ ((i & 1) << 6)) + (i >> 1) * 4096; fa[set][i] = cat(lds_tr(a), lds_tr(a + 1024)); };
            auto rdB = [&](int set, unsigned p, int i) { fb[set][i] = lds_b128(((xa0 + ((p >> 1) & 3) * 8192) ^ ((2 * set + (i >> 1)) << 5)) + (i & 1) * 4096); };
            auto step = [&](auto set_, auto last_, unsigned pn) AI {
                constexpr int set = decltype(set_)::value; constexpr bool last = decltype(last_)::value;
                SBAR(); BARRIER(); SBAR();
                __builtin_amdgcn_s_sleep(2);
#pragma unroll
                for (int i = 0; i < 8; ++i) {
                    acc[i & 3] = MFMA(fa[set][2 * (i >> 2) + ((i >> 1) & 1)], fb[set][2 * (i >> 2) + (i & 1)], acc[i & 3]);
                    if (!last && i < 4) { rdA(set ^ 1, pn, i); rdB(set ^ 1, pn, i); }
                    SBAR();
                }
            };
            using I0 = std::integral_constant<int, 0>; using I1 = std::integral_constant<int, 1>;
            BAR0();
#pragma unroll
            for (int i = 0; i < 4; ++i) { rdA(0, 0, i); rdB(0, 0, i); }
#pragma unroll 1
            for (unsigned p = 0; p < 16; p += 2) { if (p == 12) VMCNT(0); step(I0{}, F_{}, p + 1); step(I1{}, F_{}, p + 2); }
            step(I0{}, F_{}, 17); step(I1{}, T_{}, 0);
#pragma unroll
            for (int s = 0; s < 8; ++s) {
                u32x4 f;
#pragma unroll
                for (int e = 0; e < 4; ++e) f[e] = cvtpk(acc[s >> 1][8 * (s & 1) + 2 * e] * QSCALE, acc[s >> 1][8 * (s & 1) + 2 * e + 1] * QSCALE);
                lds_w128(L0 + SCR + (((w4 & 2) + ((s >> 1) & 1)) * 8 + (w4 & 1) * 4 + (s >> 2) * 2 + (s & 1)) * 1024 + lane * 16, f);
            }
            BARRIER();
#pragma unroll
            for (int s = 0; s < 8; ++s) qf[s] = lds_b128(L0 + SCR + (w4 * 8 + s) * 1024 + lane * 16);
        }
    }

    const unsigned kr0 = L0 + KR + r32 * 256 + ((hi ^ (r32 & 15)) << 4);
    const unsigned vr0 = L0 + VR + (4 * hi + q4) * 256 + (q4 << 6) + g1 * 32 + p4 * 8;
    const char* Wob = Wo + ((size_t)(kvh * 256 + w4 * 64) * DM + sp * 288) * 2;
    const unsigned ov0 = (lane >> 2) * (DM * 2) + (lane & 3) * 16;
    constexpr unsigned OVS = 16 * DM * 2 - 1024;
    auto pcol = [&](unsigned nb) { return sp ? (nb == 8 ? 0u : nb + 1) : nb; };
    auto dmaO1 = [&](const char* src, unsigned m, int j) {
        if (j == 0) dma1<0, 0>(src, ov0, m); else if (j == 1) dma1<0, 1024>(src, ov0 + OVS, m); else if (j == 2) dma1<0, 2048>(src, ov0 + 2 * OVS, m); else dma1<0, 3072>(src, ov0 + 3 * OVS, m);
    };
    f32x16 o[4] = {};
    f32x16 s0, s1, negm = {};
    u32x4 pf[4];
    float m = 0.f, l = 0.f;

    constexpr int LA = 6;
    auto rdK = [&](unsigned ka, int i) { return lds_b128((ka ^ ((i >> 1) << 5)) + (i & 1) * 8192); };
    auto rdV = [&](unsigned va, int i) { const unsigned a = (va ^ ((i & 3) << 6)) + (i >> 2) * 4096; return cat(lds_tr(a), lds_tr(a + 2048)); };
    auto MPH = [&](auto doqk_, auto dopv_, unsigned ksl_, unsigned vsl_) AI {
        constexpr bool DQ = decltype(doqk_)::value, DP = decltype(dopv_)::value;
        constexpr int N = 16 * (int)DQ + 16 * (int)DP, OFFV = DQ ? 16 : 0;
        const unsigned ka = kr0 + ksl_ * TILEB, va = vr0 + vsl_ * TILEB;
        bf16x8 f[N];
        auto rd = [&](int i) { if (DQ && i < 16) f[i] = rdK(ka, i); else f[i] = rdV(va, i - OFFV); };
        __builtin_amdgcn_s_setprio(1);
#pragma unroll
        for (int i = 0; i < LA; ++i) rd(i);
        SBAR();
#pragma unroll
        for (int i = 0; i < N; ++i) {
            if (i + LA < N) rd(i + LA);
            if (DQ && i < 16) {
                if (i == 0) s0 = mfma_c(f[i], qf[0], negm); else if (i == 1) s1 = mfma_c(f[i], qf[0], negm);
                else if (i & 1) s1 = MFMA(f[i], qf[i >> 1], s1); else s0 = MFMA(f[i], qf[i >> 1], s0);
            } else { const int j = i - OFFV; o[j & 3] = MFMA(f[i], __builtin_bit_cast(bf16x8, pf[j >> 2]), o[j & 3]); }
            SBAR();
        }
        __builtin_amdgcn_s_setprio(0);
    };
    auto RESCALE = [&](auto first_) AI {
        constexpr bool first = decltype(first_)::value;
        float a = MX3(s0[0], s0[1], s1[0]), c = MX3(s0[2], s0[3], s1[1]); a = MX3(a, s1[2], s1[3]);
#pragma unroll
        for (int r = 4; r < 16; r += 4) { a = MX3(a, s0[r], s0[r + 1]); c = MX3(c, s0[r + 2], s0[r + 3]); a = MX3(a, s1[r], s1[r + 1]); c = MX3(c, s1[r + 2], s1[r + 3]); }
        const float rm = swapmax(__builtin_fmaxf(a, c));
        const float dl = first ? rm : __builtin_fmaxf(rm, 0.f);
        m += dl;
#pragma unroll
        for (int r = 0; r < 16; ++r) { s0[r] = fsub_s(s0[r], dl); s1[r] = fsub_s(s1[r], dl); negm[r] = fsub_s(negm[r], dl); }
        if (!first) {
            const float f = __builtin_amdgcn_exp2f(-dl);
            l *= f;
#pragma unroll
            for (int db = 0; db < 4; ++db) o[db] *= f;
        }
    };
    auto EXPS = [&](auto&& hook) AI {
        float sa = 0.f, sb = 0.f;
#pragma unroll
        for (int k = 0; k < 8; ++k) {
            const f32x16& s = k < 4 ? s0 : s1; const int r0 = (k & 3) * 4;
            const float e0 = __builtin_amdgcn_exp2f(s[r0]), e1 = __builtin_amdgcn_exp2f(s[r0 + 1]), e2 = __builtin_amdgcn_exp2f(s[r0 + 2]), e3 = __builtin_amdgcn_exp2f(s[r0 + 3]);
            if (k == 0) { sa = e0 + e2; sb = e1 + e3; } else { sa += e0; sb += e1; sa += e2; sb += e3; }
            unsigned w0 = cvtpk(e0, e1), w1 = cvtpk(e2, e3);
            asm volatile("" : "+v"(w0), "+v"(w1), "+v"(sa), "+v"(sb));
            pf[k >> 1][(k & 1) * 2] = w0; pf[k >> 1][(k & 1) * 2 + 1] = w1;
            SBAR(); hook(k); SBAR();
        }
        return sa + sb;
    };
    auto SM = [&](auto first_, auto&& hook) AI {
        constexpr bool first = decltype(first_)::value;
        if (first) RESCALE(first_);
        float ts = EXPS(hook);
        if (!first && __builtin_expect(__any(!(ts <= 256.f)), 0)) { RESCALE(F_{}); ts = EXPS([](int) {}); }
        l += ts;
        asm volatile("" : "+v"(l));
    };
    auto inc2 = [](unsigned s) { return s >= 3 ? s - 3 : s + 2; };
    unsigned t = g;
    unsigned kq = g, vq = g;
    const char* ksrc; const char* vsrc;
    unsigned mK, mV;
    auto hookKV = [&](int j) {
        if (j == 0) dma2<1, 0>(ksrc, kv0, kv1, mK); else if (j == 2) dma2<1, 2048>(ksrc, kv2, kv3, mK);
        else if (j == 4) dma2<1, 0>(vsrc, vvoff, vvoff, mV); else if (j == 6) dma2<1, 2048>(vsrc, vvoff, vvoff, mV);
    };
    auto prep = [&]() { ksrc = Kb + ((t + 4 + rot) & 15) * TS; vsrc = Vb + ((t + 2 + rot) & 15) * TS; mK = Lq + KR + (kq ? kq - 1 : 4) * TILEB; mV = Lq + VR + inc2(vq) * TILEB; asm volatile("" : "+s"(mK), "+s"(mV), "+s"(ksrc), "+s"(vsrc)); };
    unsigned tnext = (unsigned)__builtin_amdgcn_s_memrealtime();
    asm volatile("" : "+s"(tnext));
    if (g) { SBAR(); BARRIER(); SBAR(); }
    prep();
    MPH(T_{}, F_{}, kq, 0);
    SBAR();
    if (g) VMCNT(8);
    BARRIER(); SBAR();
    SM(T_{}, hookKV); SBAR();
    VMCNT(8);
#pragma unroll 1
    while (t <= 9) {
        const unsigned vcur = vq;
        kq = inc2(kq); vq = inc2(vq); t += 2; prep();
        SBAR(); BARRIER(); SBAR();
        MPH(T_{}, T_{}, kq, vcur);
        SBAR(); BARRIER(); SBAR();
        tnext = pace(tnext, 240); SBAR();
        SM(F_{}, hookKV); SBAR();
        VMCNT(8);
    }
#pragma unroll 1
    while (t <= 13) {
        const unsigned vcur = vq;
        kq = inc2(kq); vq = inc2(vq); t += 2; prep();
        const char* oA = Wob + pcol(t == 12 ? 8 : 2 * t - 26) * 64; const char* oB = Wob + pcol(2 * t - 25) * 64;
        unsigned mA = Lq + KR + (t == 12 ? 1 : kq) * TILEB, mB = Lq + VR + (t - 12) * TILEB;
        asm volatile("" : "+s"(oA), "+s"(oB), "+s"(mA), "+s"(mB));
        SBAR(); BARRIER(); SBAR();
        MPH(T_{}, T_{}, kq, vcur);
        SBAR(); BARRIER(); SBAR();
        if (t <= 13) { hookKV(4); hookKV(6); }
        SBAR();
        SM(F_{}, [&](int j) { if (!(j & 1)) { dmaO1(oA, mA, j >> 1); if (t >= 13) dmaO1(oB, mB, j >> 1); } }); SBAR();
        if (t == 13 || t == 14) VMCNT(12); else VMCNT(8);
    }
    SBAR(); BARRIER(); SBAR();
    MPH(F_{}, T_{}, 0, vq);
    l = swapadd(l);

    const unsigned wor = (4 * hi + q4) * 64 + g1 * 32 + p4 * 8;
    bf16x8 wf[16];
    auto xslot = [](unsigned w) { return (w & 1) ? VR + (1 + (w >> 1)) * TILEB : KR + (3 + (w >> 1)) * TILEB; };
    u64 tag;
    auto TAIL = [&](auto G_) AI {
        constexpr int G = decltype(G_)::value;
        const __amdgpu_buffer_rsrc_t p1o = __builtin_amdgcn_make_buffer_rsrc(ws + WS_P1 + (size_t)(me * 8 + wid) * 4608, 0, 4608, 0x00020000);
        const __amdgpu_buffer_rsrc_t p1i = __builtin_amdgcn_make_buffer_rsrc(ws + WS_P1 + (size_t)((me ^ 1) * 8 + wid) * 4608, 0, 4608, 0x00020000);
        const unsigned xa = L0 + xslot(w4) + lane * 16, xb = L0 + ((w4 & 2) ? VR + 4 * TILEB : KR + 2 * TILEB) + (w4 & 1) * 8192 + lane * 16;
        const float inv = __builtin_amdgcn_rcpf(l);
        float lse = m + __builtin_amdgcn_logf(l);
        u32x4 f[4];
#pragma unroll
        for (int j = 0; j < 4; ++j) {
#pragma unroll
            for (int e = 0; e < 4; ++e) { const int r = 8 * (j & 1) + 2 * e; const f32x16& oo = o[2 * (G ^ 1) + (j >> 1)]; f[j][e] = cvtpk(oo[r] * inv, oo[r + 1] * inv); }
            asm volatile("" : "+v"(f[j]));
        }
        if (!G) {
            asm volatile("" : "+v"(lse));
            SBAR(); VMCNT(0);
            BARRIER(); SBAR();
#pragma unroll
            for (int ks = 0; ks < 16; ++ks) wf[ks] = cat(lds_tr(L0 + xslot(w4) + wor + ks * 1024), lds_tr(L0 + xslot(w4) + wor + ks * 1024 + 512));
            asm volatile("s_waitcnt lgkmcnt(0)" ::: "memory");
        }
        const unsigned xo = G ? xb : xa, xi = G ? xa : xb;
#pragma unroll
        for (int j = 0; j < 4; ++j) lds_w128(xo + j * 1024, f[j]);
        *(LAS float*)(uintptr_t)(xo - lane * 12 + 4096) = lse;
        if (!G) {
#pragma unroll
            for (int j = 0; j < 4; ++j) { dmaO1(Wob + pcol(6) * 64, L0 + xslot(w4) + 8192, j); dmaO1(Wob + pcol(7) * 64, L0 + xslot(w4) + 12288, j); }
        }
        BARRIER();
        const u64 epoch = __hip_atomic_load((GAS u64*)(ws + WS_EPOCH + b * 256), RLX_AGENT);
        const float lseO = *(const LAS float*)(uintptr_t)(xi - lane * 12 + 4096);
#pragma unroll
        for (int j = 0; j < 4; ++j) f[j] = *(const LAS u32x4*)(uintptr_t)(xi + j * 1024);
        const float mx = __builtin_fmaxf(lse, lseO), ea = __builtin_amdgcn_exp2f(lse - mx), eb = __builtin_amdgcn_exp2f(lseO - mx);
        const float rden = __builtin_amdgcn_rcpf(ea + eb), ca = ea * rden * inv, cb = eb * rden, lseL = mx + __builtin_amdgcn_logf(ea + eb);
        __builtin_amdgcn_raw_buffer_store_b32(__float_as_uint(lseL), p1o, 4096 + lane * 4, 0, 16);
#pragma unroll
        for (int j = 0; j < 4; ++j) {
            u32x4 w;
#pragma unroll
            for (int e = 0; e < 4; ++e) {
                const int r = 8 * (j & 1) + 2 * e; f32x16& oo = o[2 * G + (j >> 1)];
                oo[r] = oo[r] * ca + bflo(f[j][e]) * cb; oo[r + 1] = oo[r + 1] * ca + bfhi(f[j][e]) * cb;
                w[e] = cvtpk(oo[r], oo[r + 1]);
            }
            st_sc1(p1o, j * 1024 + lane * 16, w);
        }
        SBAR(); VMCNT(0); SBAR();
        tag = ((u64)__builtin_amdgcn_readfirstlane((unsigned)(epoch >> 32)) << 32 | (unsigned)__builtin_amdgcn_readfirstlane((unsigned)epoch)) * 0x9E3779B97F4A7C15ull + 0x632BE59BD9B4E019ull;
        if (lane == 0) __hip_atomic_store((GAS u64*)(ws + WS_F1 + (me * 8 + wid) * 64), tag, RLX_AGENT);
        BARRIER();
        if (G) {
#pragma unroll
            for (int ks = 0; ks < 16; ++ks) {
                const unsigned a = L0 + (w4 < 2 ? (w4 ? VR + 3 * TILEB : KR) + ks * 1024 : xslot(ks >> 2) + w4 * 4096 + (ks & 3) * 1024) + wor;
                wf[ks] = cat(lds_tr(a), lds_tr(a + 512));
            }
        }
        wait_flag((GAS u64*)(ws + WS_F1 + ((me ^ 1) * 8 + wid) * 64), tag);
        const float lseR = __uint_as_float(__builtin_amdgcn_raw_buffer_load_b32(p1i, 4096 + lane * 4, 0, 16));
#pragma unroll
        for (int j = 0; j < 4; ++j) f[j] = ld_sc1(p1i, j * 1024 + lane * 16);
        const float mx2 = __builtin_fmaxf(lseL, lseR), e0 = __builtin_amdgcn_exp2f(lseL - mx2), e1 = __builtin_amdgcn_exp2f(lseR - mx2);
        const float rd2 = __builtin_amdgcn_rcpf(e0 + e1), c0 = e0 * rd2, c1 = e1 * rd2;
#pragma unroll
        for (int j = 0; j < 4; ++j) {
            u32x4 w;
#pragma unroll
            for (int e = 0; e < 4; ++e) {
                const int r = 8 * (j & 1) + 2 * e; const f32x16& oo = o[2 * G + (j >> 1)];
                w[e] = cvtpk(oo[r] * c0 + bflo(f[j][e]) * c1, oo[r + 1] * c0 + bfhi(f[j][e]) * c1);
            }
            lds_w128(xa + (4 * G + j) * 1024, w);
        }
        BARRIER();
    };
    if (g) TAIL(std::integral_constant<int, 1>{}); else TAIL(std::integral_constant<int, 0>{});
    {
        const __amdgpu_buffer_rsrc_t p2 = __builtin_amdgcn_make_buffer_rsrc(ws + WS_P2 + (size_t)(b * 2 + sp) * P2STRIDE, 0, 16 * 36864, 0x00020000);
        auto put = [&](const f32x16& c, unsigned piece) {
#pragma unroll
            for (int u = 0; u < 2; ++u) {
                u32x4 f;
#pragma unroll
                for (int e = 0; e < 4; ++e) f[e] = cvtpk(c[8 * u + 2 * e], c[8 * u + 2 * e + 1]);
                st_sc1(p2, kvh * 36864 + (piece + u) * 1024 + lane * 16, f);
            }
        };
        constexpr int LB = 6;
        const unsigned rbf = wid & 1;
        const unsigned ob[4] = {L0 + xslot(rbf) + lane * 16, L0 + xslot(2 + rbf) + lane * 16, L0 + xslot(rbf ^ 1) + lane * 16, L0 + xslot(2 + (rbf ^ 1)) + lane * 16};
        {
            bf16x8 f[32]; f32x16 c = {}, c2 = {};
            auto rd = [&](int i) { f[i] = lds_b128(ob[i >> 3] + (i & 7) * 1024); };
#pragma unroll
            for (int i = 0; i < LB; ++i) rd(i);
            SBAR();
#pragma unroll
            for (int i = 0; i < 32; ++i) {
                if (i + LB < 32) rd(i + LB);
                if (i < 16) c = MFMA(wf[i], f[i], c); else c2 = MFMA(wf[i - 16], f[i], c2);
                if (i == 19) put(c, wid * 4 + rbf * 2);
                SBAR();
            }
            if (wid < 2) {
                bf16x8 fa[16]; c = f32x16{};
                auto rd8 = [&](int i) { const unsigned a = L0 + KR + 1 * TILEB + wor + i * 1024; fa[i] = cat(lds_tr(a), lds_tr(a + 512)); f[i] = lds_b128(ob[i >> 3] + (i & 7) * 1024); };
#pragma unroll
                for (int i = 0; i < 4; ++i) rd8(i);
                SBAR();
#pragma unroll
                for (int i = 0; i < 16; ++i) {
                    if (i + 4 < 16) rd8(i + 4);
                    c = MFMA(fa[i], f[i], c);
                    if (i == 3) put(c2, wid * 4 + (rbf ^ 1) * 2);
                    SBAR();
                }
                put(c, 32 + wid * 2);
            } else put(c2, wid * 4 + (rbf ^ 1) * 2);
        }
        VMCNT(0);
        GAS u64* f2 = (GAS u64*)(ws + WS_F2);
        {
            const unsigned cons = lane < 2 ? wid * 2 + lane : wid * 8 + (lane - 2);
            if (lane < (wid < 2 ? 10u : 2u)) __hip_atomic_store(f2 + (size_t)((b * HKV + cons) * 2 + sp) * 32 + (lane < 2 ? 0 : 16) + kvh, tag, RLX_AGENT);
        }
        {
            if (wid == 7) wait_flag(f2 + (size_t)me * 32 + (lane & 31), tag);
            const unsigned sg = lane >> 4, c16 = lane & 15;
            const unsigned cc = kvh * 128 + wid * 16 + c16, c8 = 2048 + kvh * 16 + c16;
            auto oaddr = [&](unsigned c) {
                const unsigned piece = c >> 6, ln = c & 63, nb = piece >> 2, rb = (piece >> 1) & 1, u = piece & 1;
                return Out + ((size_t)(b * 64 + rb * 32 + (ln & 31)) * DM + sp * 288 + pcol(nb) * 32 + u * 16 + (ln >> 5) * 4 + (sg >> 1) * 8 + (sg & 1) * 2) * 2;
            };
            char* op = oaddr(cc); char* op8 = oaddr(c8);
            asm volatile("" : "+v"(op), "+v"(op8));
            BAR0();
            auto fin = [&](const u32x4* v, char* o_) {
                float a[8];
#pragma unroll
                for (int e = 0; e < 4; ++e) { a[2 * e] = (bflo(v[0][e]) + bflo(v[1][e])) + (bflo(v[2][e]) + bflo(v[3][e])); a[2 * e + 1] = (bfhi(v[0][e]) + bfhi(v[1][e])) + (bfhi(v[2][e]) + bfhi(v[3][e])); }
                float h[4], q[2];
#pragma unroll
                for (int e = 0; e < 4; ++e) { auto r = __builtin_amdgcn_permlane32_swap(__float_as_uint(a[e]), __float_as_uint(a[e + 4]), false, false); h[e] = __uint_as_float(r[0]) + __uint_as_float(r[1]); }
#pragma unroll
                for (int e = 0; e < 2; ++e) { auto r = __builtin_amdgcn_permlane16_swap(__float_as_uint(h[e]), __float_as_uint(h[e + 2]), false, false); q[e] = __uint_as_float(r[0]) + __uint_as_float(r[1]); }
                *(GAS unsigned*)o_ = cvtpk(q[0], q[1]);
            };
            u32x4 v[4], v8[4];
#pragma unroll
            for (int s = 0; s < 4; ++s) v[s] = ld_sc1(p2, (sg * 4 + s) * 36864 + cc * 16);
            if (wid == 6) {
#pragma unroll
                for (int s = 0; s < 4; ++s) v8[s] = ld_sc1(p2, (sg * 4 + s) * 36864 + c8 * 16);
            }
            if (wid == 0 && kvh == 0 && sp == 0 && lane == 0) __hip_atomic_store((GAS u64*)(ws + WS_EPOCH + b * 256), tag, RLX_AGENT);
            fin(v, op);
            if (wid == 6) fin(v8, op8);
        }
    }
}

extern "C" void kernel_launch(void* const* d_in, const int* in_sizes, int n_in, void* d_out, int out_size, void* d_ws, size_t ws_size, hipStream_t stream) {
    (void)hipFuncSetAttribute((const void*)gqa_fused, hipFuncAttributeMaxDynamicSharedMemorySize, LDS_BYTES);
    hipLaunchKernelGGL(gqa_fused, dim3(256), dim3(512), LDS_BYTES, stream, (const char*)d_in[0], (const char*)d_in[1], (const char*)d_in[2], (const char*)d_in[3],
                       (const char*)d_in[4], (char*)d_out, (char*)d_ws);
}
```

```cpp
#define MULT 0
#include <hip/hip_runtime.h>
#include <stdint.h>
#include <type_traits>

#define DI __device__ __forceinline__
#define LAS __attribute__((address_space(3)))
#define GAS __attribute__((address_space(1)))
typedef __bf16 bf16x8 __attribute__((ext_vector_type(8)));
typedef __bf16 bf16x2 __attribute__((ext_vector_type(2)));
typedef short s16x4 __attribute__((ext_vector_type(4)));
typedef short s16x8 __attribute__((ext_vector_type(8)));
typedef float f32x2 __attribute__((ext_vector_type(2)));
typedef float f32x16 __attribute__((ext_vector_type(16)));
typedef unsigned u32x4 __attribute__((ext_vector_type(4)));
typedef unsigned u32x2 __attribute__((ext_vector_type(2)));
typedef unsigned long long u64;

constexpr int SQ = 64, DM = 576, HKV = 16, SK = 2048;
constexpr unsigned TILEB = 16384;
constexpr unsigned KR = 0, VR = 5 * TILEB;
constexpr unsigned SCR = 8 * TILEB;
constexpr unsigned LDS_BYTES = 10 * TILEB;
constexpr unsigned CODE_BYTES = 25600;
constexpr float QSCALE = 0.08838834764831845f * 1.4426950408889634f;

#define AI __attribute__((always_inline))
#define SBAR() __builtin_amdgcn_sched_barrier(0)
#define MFMA(a, b, c) __builtin_amdgcn_mfma_f32_32x32x16_bf16((a), (b), (c), 0, 0, 0)
#define VMCNT(n) asm volatile("s_waitcnt vmcnt(%0)" :: "i"(n) : "memory")
#define BARRIER() asm volatile("s_waitcnt lgkmcnt(0)\n\ts_barrier" ::: "memory")
#define BAR0() asm volatile("s_barrier" ::: "memory")
#define RLX_AGENT __ATOMIC_RELAXED, __HIP_MEMORY_SCOPE_AGENT

DI unsigned cvtpk(float lo, float hi) { f32x2 v = {lo, hi}; bf16x2 b = __builtin_convertvector(v, bf16x2); return __builtin_bit_cast(unsigned, b); }
DI float bflo(unsigned u) { return __uint_as_float(u << 16); }
DI float bfhi(unsigned u) { return __uint_as_float(u & 0xffff0000u); }
DI bf16x8 lds_b128(unsigned a) { return *(const LAS bf16x8*)(uintptr_t)a; }
DI s16x4 lds_tr(unsigned a) { return __builtin_amdgcn_ds_read_tr16_b64_v4i16((LAS s16x4*)(uintptr_t)a); }
DI bf16x8 cat(s16x4 lo, s16x4 hi) { s16x8 r = __builtin_shufflevector(lo, hi, 0, 1, 2, 3, 4, 5, 6, 7); return __builtin_bit_cast(bf16x8, r); }
DI void lds_w128(unsigned a, u32x4 v) { *(LAS u32x4*)(uintptr_t)a = v; }
template <int NT, int OFF> DI void dma1(const void* sbase, unsigned voff, unsigned m) {
    if constexpr (NT) asm volatile("s_mov_b32 m0, %2\n\ts_nop 0\n\tglobal_load_lds_dwordx4 %0, %1 offset:%3 nt" :: "v"(voff), "s"(sbase), "s"(m), "i"(OFF) : "memory");
    else asm volatile("s_mov_b32 m0, %2\n\ts_nop 0\n\tglobal_load_lds_dwordx4 %0, %1 offset:%3" :: "v"(voff), "s"(sbase), "s"(m), "i"(OFF) : "memory");
}
template <int NT, int OFF> DI void dma2(const void* sbase, unsigned v0, unsigned v1, unsigned m) {
    if constexpr (NT) asm volatile("s_mov_b32 m0, %3\n\ts_nop 0\n\tglobal_load_lds_dwordx4 %0, %2 offset:%4 nt\n\tglobal_load_lds_dwordx4 %1, %2 offset:%5 nt" :: "v"(v0), "v"(v1), "s"(sbase), "s"(m), "i"(OFF), "i"(OFF + 1024) : "memory");
    else asm volatile("s_mov_b32 m0, %3\n\ts_nop 0\n\tglobal_load_lds_dwordx4 %0, %2 offset:%4\n\tglobal_load_lds_dwordx4 %1, %2 offset:%5" :: "v"(v0), "v"(v1), "s"(sbase), "s"(m), "i"(OFF), "i"(OFF + 1024) : "memory");
}
DI f32x16 mfma_c(bf16x8 a, bf16x8 b, const f32x16& c) {
    f32x16 d; asm("s_nop 1\n\tv_mfma_f32_32x32x16_bf16 %0, %1, %2, %3" : "=&v"(d) : "v"(a), "v"(b), "v"(c)); return d; }
DI float fsub_s(float a, float b) { float r; asm("v_sub_f32_e32 %0, %1, %2" : "=v"(r) : "v"(a), "v"(b)); return r; }
DI float swapmax(float x) { auto r = __builtin_amdgcn_permlane32_swap(__float_as_uint(x), __float_as_uint(x), false, false); return __builtin_fmaxf(__uint_as_float(r[0]), __uint_as_float(r[1])); }
DI float swapadd(float x) { auto r = __builtin_amdgcn_permlane32_swap(__float_as_uint(x), __float_as_uint(x), false, false); return __uint_as_float(r[0]) + __uint_as_float(r[1]); }
#define MX3(a, b, c) __builtin_fmaxf(__builtin_fmaxf((a), (b)), (c))
DI void st_sc1(__amdgpu_buffer_rsrc_t r, unsigned off, u32x4 v) { __builtin_amdgcn_raw_buffer_store_b128(v, r, off, 0, 16); }
DI u32x4 ld_sc1(__amdgpu_buffer_rsrc_t r, unsigned off) { return __builtin_amdgcn_raw_buffer_load_b128(r, off, 0, 16); }
DI void wait_flag(GAS u64* f, u64 tag) {
    u64 v0 = __hip_atomic_load(f, RLX_AGENT);
    if (!__all(v0 == tag)) {
        v0 = __hip_atomic_load(f, RLX_AGENT);
        __builtin_amdgcn_s_sleep(8);
#pragma unroll 1
        for (unsigned spin = 0; spin < (1u << 20); ++spin) {
            const u64 v1 = __hip_atomic_load(f, RLX_AGENT);
            if (__all(v0 == tag)) break;
            v0 = __hip_atomic_load(f, RLX_AGENT);
            if (__all(v1 == tag)) break;
        }
    }
    asm volatile("" ::: "memory");
}

DI unsigned pace(unsigned tn, unsigned per) {
    unsigned nx;
    asm volatile("1:\n\ts_memrealtime s[98:99]\n\ts_waitcnt lgkmcnt(0)\n\ts_sub_i32 s99, s98, %1\n\ts_cmp_lt_i32 s99, 0\n\ts_cbranch_scc1 1b\n\ts_add_i32 %0, s98, %2"
                 : "=s"(nx) : "s"(tn), "s"(per) : "memory", "scc", "s98", "s99");
    return nx;
}

constexpr size_t WS_EPOCH = 0;
constexpr size_t WS_F1 = 65536;
constexpr size_t WS_F2 = 262144;
constexpr size_t WS_P1 = 1u << 20;
constexpr size_t WS_P2 = 12u << 20;
constexpr unsigned P2STRIDE = 16 * 36864;

using T_ = std::integral_constant<bool, true>; using F_ = std::integral_constant<bool, false>;

__global__ __launch_bounds__(512, 2) void gqa_fused(const char* X, const char* Kc, const char* Vc, const char* Wq, const char* Wo, char* Out, char* ws) {
    extern __shared__ __attribute__((aligned(16))) char smem[];
    u64 tm0, tm1;
    const char* ka = (const char*)__builtin_amdgcn_kernarg_segment_ptr();
    unsigned dmy;
    asm volatile("s_memrealtime %0\n\ts_waitcnt lgkmcnt(0)\n\ts_load_dword %2, %3, 0x0\n\ts_waitcnt lgkmcnt(0)\n\ts_memrealtime %1\n\ts_waitcnt lgkmcnt(0)" : "=&s"(tm0), "=&s"(tm1), "=&s"(dmy) : "s"(ka) : "memory");
    typedef const __attribute__((address_space(4))) u64* cp;
    asm volatile("" : "+s"(ka));
    X = (const char*)((cp)ka)[0]; Kc = (const char*)((cp)ka)[1]; Vc = (const char*)((cp)ka)[2]; Wq = (const char*)((cp)ka)[3]; Wo = (const char*)((cp)ka)[4]; Out = (char*)((cp)ka)[5]; ws = (char*)((cp)ka)[6];
    asm volatile("" : "+s"(X), "+s"(Kc), "+s"(Vc), "+s"(Wq), "+s"(Wo), "+s"(Out), "+s"(ws));
    u64 pc0; asm volatile("s_getpc_b64 %0" : "=s"(pc0));
    const unsigned tid = threadIdx.x, lane = tid & 63;
    const unsigned wid = __builtin_amdgcn_readfirstlane(tid >> 6);
    const unsigned r32 = lane & 31, hi = lane >> 5, w4 = wid & 3, g = wid >> 2;
    const unsigned L0 = (unsigned)(uintptr_t)smem;
    const unsigned wg = blockIdx.x, xcd = wg & 7, slot = wg >> 3;
    const unsigned kvh = (xcd & 3) * 4 + (slot & 3), sp = xcd >> 2, b = slot >> 2;
    const unsigned pair = b * HKV + kvh, me = pair * 2 + sp;
    constexpr unsigned TS = 16384;
    const unsigned woff = sp * 262144 + w4 * 4096;
    const char* Kb = Kc + (size_t)pair * SK * 256 + woff;
    const char* Vb = Vc + (size_t)pair * SK * 256 + woff;
    const unsigned kv0 = (lane >> 4) * 256 + (((lane & 15) ^ (lane >> 4)) << 4), kv1 = kv0 ^ 64, kv2 = kv0 ^ 128, kv3 = kv0 ^ 192;
    const unsigned vvoff = (lane >> 4) * 256 + (((lane & 15) ^ ((lane >> 4) << 2)) << 4);
    const unsigned Lw = L0 + w4 * 1024, Lq = L0 + w4 * 4096;
    const unsigned rot = b * 2;
    auto dmaK = [&](unsigned t, unsigned sl) { t = (t + rot) & 15; dma2<1, 0>(Kb + t * TS, kv0, kv1, Lq + KR + sl * TILEB); dma2<1, 2048>(Kb + t * TS, kv2, kv3, Lq + KR + sl * TILEB); };
    auto dmaV = [&](unsigned t, unsigned sl) { t = (t + rot) & 15; dma2<1, 0>(Vb + t * TS, vvoff, vvoff, Lq + VR + sl * TILEB); dma2<1, 2048>(Vb + t * TS, vvoff, vvoff, Lq + VR + sl * TILEB); };
    const unsigned g1 = (lane >> 4) & 1, q4 = (lane & 15) >> 2, p4 = lane & 3;

    bf16x8 qf[8];
    auto touch = [&](const void* sbase, unsigned voff, unsigned dst) {
        asm volatile("s_mov_b32 m0, %2\n\ts_nop 0\n\tglobal_load_lds_dword %0, %1" :: "v"(voff), "s"(sbase), "s"(dst) : "memory");
    };
    {
        constexpr unsigned XR = 3 * TILEB, WR = 6 * TILEB;
        if (g) {
            const char* Xb = X + (size_t)b * SQ * DM * 2;
            const unsigned xvoff = (8 * w4 + (lane >> 3)) * (DM * 2) + (((lane & 7) ^ (4 * (w4 & 1) + (lane >> 4))) << 4);
            const char* Wb = Wq + (2 * kvh + (w4 >> 1)) * 256 + (size_t)(w4 & 1) * 16 * 8192;
            const unsigned wvoff = (lane >> 4) * 8192 + (((lane & 15) ^ ((lane >> 4) << 2)) << 4);
            unsigned cb = b + (kvh & 3) * 2; cb = cb >= 9 ? cb - 9 : cb;
            unsigned xb = cb;
            auto xhalf = [&](unsigned j, unsigned h) { dma1<0, 0>(Xb + xb * 128 + h * (32 * DM * 2), xvoff, Lw + XR + (j & 3) * 8192 + h * 4096); if (h) xb = xb == 8 ? 0 : xb + 1; };
            auto phase = [&](unsigned p) {
                xhalf((p + 1) >> 1, (p & 1) ^ 1);
                const char* src = Wb + (size_t)(cb * 2 + (p & 1)) * (32 * 8192);
                dma2<0, 0>(src, wvoff, wvoff + 31744, Lq + WR + (p & 3) * TILEB); dma2<0, 2048>(src, wvoff + 2 * 31744, wvoff + 3 * 31744, Lq + WR + (p & 3) * TILEB);
                if (p & 1) cb = cb == 8 ? 0 : cb + 1;
            };
            for (unsigned i = 0; i < (b & 3) * 4; ++i) __builtin_amdgcn_s_sleep(4);
            xhalf(0, 0);
            phase(0); phase(1); phase(2);
            VMCNT(10); BAR0();
            phase(3);
#pragma unroll 1
            for (unsigned p = 4; p < 18; ++p) { VMCNT(10); BAR0(); phase(p); }
            VMCNT(10); BAR0(); dmaK(0, 0);
            VMCNT(9); BAR0(); dmaK(1, 1);
            VMCNT(8); BAR0(); dmaK(2, 2);
            BAR0();
            dmaV(0, 0);
            VMCNT(12); BAR0();
#pragma unroll
            for (int s = 0; s < 8; ++s) qf[s] = lds_b128(L0 + SCR + (w4 * 8 + s) * 1024 + lane * 16);
            SBAR();
            dmaK(3, 3); dmaV(1, 1);
            VMCNT(16);
        } else {
            if (w4 == 0) { const unsigned ln = slot * 8 + (lane & 7); touch((const void*)(pc0 & ~127ull), ln * 128 < CODE_BYTES ? ln * 128 : 0u, Lw + VR); }
            const unsigned ps = ((p4 & 1) << 1) | (p4 >> 1);
            const unsigned wa0 = (L0 + WR + (w4 >> 1) * 8192 + (8 * hi + q4) * 256 + (q4 << 6) + g1 * 32 + ps * 8) ^ ((w4 & 1) << 7);
            const unsigned xa0 = L0 + XR + r32 * 128 + ((hi ^ ((r32 >> 1) & 7)) << 4);
            f32x16 acc[4] = {};
            bf16x8 fa[2][4], fb[2][4];
            auto rdA = [&](int set, unsigned p, int i) { const unsigned a = ((wa0 + (p & 3) * TILEB) ^ ((i & 1) << 6)) + (i >> 1) * 4096; fa[set][i] = cat(lds_tr(a), lds_tr(a + 1024)); };
            auto rdB = [&](int set, unsigned p, int i) { fb[set][i] = lds_b128(((xa0 + ((p >> 1) & 3) * 8192) ^ ((2 * set + (i >> 1)) << 5)) + (i & 1) * 4096); };
            auto step = [&](auto set_, auto last_, unsigned pn) AI {
                constexpr int set = decltype(set_)::value; constexpr bool last = decltype(last_)::value;
                SBAR(); BARRIER(); SBAR();
                __builtin_amdgcn_s_sleep(2);
#pragma unroll
                for (int i = 0; i < 8; ++i) {
                    acc[i & 3] = MFMA(fa[set][2 * (i >> 2) + ((i >> 1) & 1)], fb[set][2 * (i >> 2) + (i & 1)], acc[i & 3]);
                    if (!last && i < 4) { rdA(set ^ 1, pn, i); rdB(set ^ 1, pn, i); }
                    SBAR();
                }
            };
            using I0 = std::integral_constant<int, 0>; using I1 = std::integral_constant<int, 1>;
            BAR0();
#pragma unroll
            for (int i = 0; i < 4; ++i) { rdA(0, 0, i); rdB(0, 0, i); }
#pragma unroll 1
            for (unsigned p = 0; p < 16; p += 2) { if (p == 12) VMCNT(0); step(I0{}, F_{}, p + 1); step(I1{}, F_{}, p + 2); }
            step(I0{}, F_{}, 17); step(I1{}, T_{}, 0);
#pragma unroll
            for (int s = 0; s < 8; ++s) {
                u32x4 f;
#pragma unroll
                for (int e = 0; e < 4; ++e) f[e] = cvtpk(acc[s >> 1][8 * (s & 1) + 2 * e] * QSCALE, acc[s >> 1][8 * (s & 1) + 2 * e + 1] * QSCALE);
                lds_w128(L0 + SCR + (((w4 & 2) + ((s >> 1) & 1)) * 8 + (w4 & 1) * 4 + (s >> 2) * 2 + (s & 1)) * 1024 + lane * 16, f);
            }
            BARRIER();
#pragma unroll
            for (int s = 0; s < 8; ++s) qf[s] = lds_b128(L0 + SCR + (w4 * 8 + s) * 1024 + lane * 16);
        }
    }

    const unsigned kr0 = L0 + KR + r32 * 256 + ((hi ^ (r32 & 15)) << 4);
    const unsigned vr0 = L0 + VR + (4 * hi + q4) * 256 + (q4 << 6) + g1 * 32 + p4 * 8;
    const char* Wob = Wo + ((size_t)(kvh * 256 + w4 * 64) * DM + sp * 288) * 2;
    const unsigned ov0 = (lane >> 2) * (DM * 2) + (lane & 3) * 16;
    constexpr unsigned OVS = 16 * DM * 2 - 1024;
    auto pcol = [&](unsigned nb) { return sp ? (nb == 8 ? 0u : nb + 1) : nb; };
    auto dmaO1 = [&](const char* src, unsigned m, int j) {
        if (j == 0) dma1<0, 0>(src, ov0, m); else if (j == 1) dma1<0, 1024>(src, ov0 + OVS, m); else if (j == 2) dma1<0, 2048>(src, ov0 + 2 * OVS, m); else dma1<0, 3072>(src, ov0 + 3 * OVS, m);
    };
    f32x16 o[4] = {};
    f32x16 s0, s1, negm = {};
    u32x4 pf[4];
    float m = 0.f, l = 0.f;

    constexpr int LA = 6;
    auto rdK = [&](unsigned ka, int i) { return lds_b128((ka ^ ((i >> 1) << 5)) + (i & 1) * 8192); };
    auto rdV = [&](unsigned va, int i) { const unsigned a = (va ^ ((i & 3) << 6)) + (i >> 2) * 4096; return cat(lds_tr(a), lds_tr(a + 2048)); };
    auto MPH = [&](auto doqk_, auto dopv_, unsigned ksl_, unsigned vsl_) AI {
        constexpr bool DQ = decltype(doqk_)::value, DP = decltype(dopv_)::value;
        constexpr int N = 16 * (int)DQ + 16 * (int)DP, OFFV = DQ ? 16 : 0;
        const unsigned ka = kr0 + ksl_ * TILEB, va = vr0 + vsl_ * TILEB;
        bf16x8 f[N];
        auto rd = [&](int i) { if (DQ && i < 16) f[i] = rdK(ka, i); else f[i] = rdV(va, i - OFFV); };
        __builtin_amdgcn_s_setprio(1);
#pragma unroll
        for (int i = 0; i < LA; ++i) rd(i);
        SBAR();
#pragma unroll
        for (int i = 0; i < N; ++i) {
            if (i + LA < N) rd(i + LA);
            if (DQ && i < 16) {
                if (i == 0) s0 = mfma_c(f[i], qf[0], negm); else if (i == 1) s1 = mfma_c(f[i], qf[0], negm);
                else if (i & 1) s1 = MFMA(f[i], qf[i >> 1], s1); else s0 = MFMA(f[i], qf[i >> 1], s0);
            } else { const int j = i - OFFV; o[j & 3] = MFMA(f[i], __builtin_bit_cast(bf16x8, pf[j >> 2]), o[j & 3]); }
            SBAR();
        }
        __builtin_amdgcn_s_setprio(0);
    };
    auto RESCALE = [&](auto first_) AI {
        constexpr bool first = decltype(first_)::value;
        float a = MX3(s0[0], s0[1], s1[0]), c = MX3(s0[2], s0[3], s1[1]); a = MX3(a, s1[2], s1[3]);
#pragma unroll
        for (int r = 4; r < 16; r += 4) { a = MX3(a, s0[r], s0[r + 1]); c = MX3(c, s0[r + 2], s0[r + 3]); a = MX3(a, s1[r], s1[r + 1]); c = MX3(c, s1[r + 2], s1[r + 3]); }
        const float rm = swapmax(__builtin_fmaxf(a, c));
        const float dl = first ? rm : __builtin_fmaxf(rm, 0.f);
        m += dl;
#pragma unroll
        for (int r = 0; r < 16; ++r) { s0[r] = fsub_s(s0[r], dl); s1[r] = fsub_s(s1[r], dl); negm[r] = fsub_s(negm[r], dl); }
        if (!first) {
            const float f = __builtin_amdgcn_exp2f(-dl);
            l *= f;
#pragma unroll
            for (int db = 0; db < 4; ++db) o[db] *= f;
        }
    };
    auto EXPS = [&](auto&& hook) AI {
        float sa = 0.f, sb = 0.f;
#pragma unroll
        for (int k = 0; k < 8; ++k) {
            const f32x16& s = k < 4 ? s0 : s1; const int r0 = (k & 3) * 4;
            const float e0 = __builtin_amdgcn_exp2f(s[r0]), e1 = __builtin_amdgcn_exp2f(s[r0 + 1]), e2 = __builtin_amdgcn_exp2f(s[r0 + 2]), e3 = __builtin_amdgcn_exp2f(s[r0 + 3]);
            if (k == 0) { sa = e0 + e2; sb = e1 + e3; } else { sa += e0; sb += e1; sa += e2; sb += e3; }
            unsigned w0 = cvtpk(e0, e1), w1 = cvtpk(e2, e3);
            asm volatile("" : "+v"(w0), "+v"(w1), "+v"(sa), "+v"(sb));
            pf[k >> 1][(k & 1) * 2] = w0; pf[k >> 1][(k & 1) * 2 + 1] = w1;
            SBAR(); hook(k); SBAR();
        }
        return sa + sb;
    };
    auto SM = [&](auto first_, auto&& hook) AI {
        constexpr bool first = decltype(first_)::value;
        if (first) RESCALE(first_);
        float ts = EXPS(hook);
        if (!first && __builtin_expect(__any(!(ts <= 256.f)), 0)) { RESCALE(F_{}); ts = EXPS([](int) {}); }
        l += ts;
        asm volatile("" : "+v"(l));
    };
    auto inc2 = [](unsigned s) { return s >= 3 ? s - 3 : s + 2; };
    unsigned t = g;
    unsigned kq = g, vq = g;
    const char* ksrc; const char* vsrc;
    unsigned mK, mV;
    auto hookKV = [&](int j) {
        if (j == 0) dma2<1, 0>(ksrc, kv0, kv1, mK); else if (j == 2) dma2<1, 2048>(ksrc, kv2, kv3, mK);
        else if (j == 4) dma2<1, 0>(vsrc, vvoff, vvoff, mV); else if (j == 6) dma2<1, 2048>(vsrc, vvoff, vvoff, mV);
    };
    auto prep = [&]() { ksrc = Kb + ((t + 4 + rot) & 15) * TS; vsrc = Vb + ((t + 2 + rot) & 15) * TS; mK = Lq + KR + (kq ? kq - 1 : 4) * TILEB; mV = Lq + VR + inc2(vq) * TILEB; asm volatile("" : "+s"(mK), "+s"(mV), "+s"(ksrc), "+s"(vsrc)); };
    unsigned tnext = (unsigned)__builtin_amdgcn_s_memrealtime();
    asm volatile("" : "+s"(tnext));
    if (g) { SBAR(); BARRIER(); SBAR(); }
    prep();
    MPH(T_{}, F_{}, kq, 0);
    SBAR();
    if (g) VMCNT(8);
    BARRIER(); SBAR();
    SM(T_{}, hookKV); SBAR();
    VMCNT(8);
#pragma unroll 1
    while (t <= 9) {
        const unsigned vcur = vq;
        kq = inc2(kq); vq = inc2(vq); t += 2; prep();
        SBAR(); BARRIER(); SBAR();
        MPH(T_{}, T_{}, kq, vcur);
        SBAR(); BARRIER(); SBAR();
        tnext = pace(tnext, 240); SBAR();
        SM(F_{}, hookKV); SBAR();
        VMCNT(8);
    }
#pragma unroll 1
    while (t <= 13) {
        const unsigned vcur = vq;
        kq = inc2(kq); vq = inc2(vq); t += 2; prep();
        const char* oA = Wob + pcol(t == 12 ? 8 : 2 * t - 26) * 64; const char* oB = Wob + pcol(2 * t - 25) * 64;
        unsigned mA = Lq + KR + (t == 12 ? 1 : kq) * TILEB, mB = Lq + VR + (t - 12) * TILEB;
        asm volatile("" : "+s"(oA), "+s"(oB), "+s"(mA), "+s"(mB));
        SBAR(); BARRIER(); SBAR();
        MPH(T_{}, T_{}, kq, vcur);
        SBAR(); BARRIER(); SBAR();
        if (t <= 13) { hookKV(4); hookKV(6); }
        SBAR();
        SM(F_{}, [&](int j) { if (!(j & 1)) { dmaO1(oA, mA, j >> 1); if (t >= 13) dmaO1(oB, mB, j >> 1); } }); SBAR();
        if (t == 13 || t == 14) VMCNT(12); else VMCNT(8);
    }
    SBAR(); BARRIER(); SBAR();
    MPH(F_{}, T_{}, 0, vq);
    l = swapadd(l);

    const unsigned wor = (4 * hi + q4) * 64 + g1 * 32 + p4 * 8;
    bf16x8 wf[16];
    auto xslot = [](unsigned w) { return (w & 1) ? VR + (1 + (w >> 1)) * TILEB : KR + (3 + (w >> 1)) * TILEB; };
    u64 tag;
    auto TAIL = [&](auto G_) AI {
        constexpr int G = decltype(G_)::value;
        const __amdgpu_buffer_rsrc_t p1o = __builtin_amdgcn_make_buffer_rsrc(ws + WS_P1 + (size_t)(me * 8 + wid) * 4608, 0, 4608, 0x00020000);
        const __amdgpu_buffer_rsrc_t p1i = __builtin_amdgcn_make_buffer_rsrc(ws + WS_P1 + (size_t)((me ^ 1) * 8 + wid) * 4608, 0, 4608, 0x00020000);
        const unsigned xa = L0 + xslot(w4) + lane * 16, xb = L0 + ((w4 & 2) ? VR + 4 * TILEB : KR + 2 * TILEB) + (w4 & 1) * 8192 + lane * 16;
        const float inv = __builtin_amdgcn_rcpf(l);
        float lse = m + __builtin_amdgcn_logf(l);
        u32x4 f[4];
#pragma unroll
        for (int j = 0; j < 4; ++j) {
#pragma unroll
            for (int e = 0; e < 4; ++e) { const int r = 8 * (j & 1) + 2 * e; const f32x16& oo = o[2 * (G ^ 1) + (j >> 1)]; f[j][e] = cvtpk(oo[r] * inv, oo[r + 1] * inv); }
            asm volatile("" : "+v"(f[j]));
        }
        if (!G) {
            asm volatile("" : "+v"(lse));
            SBAR(); VMCNT(0);
            BARRIER(); SBAR();
#pragma unroll
            for (int ks = 0; ks < 16; ++ks) wf[ks] = cat(lds_tr(L0 + xslot(w4) + wor + ks * 1024), lds_tr(L0 + xslot(w4) + wor + ks * 1024 + 512));
            asm volatile("s_waitcnt lgkmcnt(0)" ::: "memory");
        }
        const unsigned xo = G ? xb : xa, xi = G ? xa : xb;
#pragma unroll
        for (int j = 0; j < 4; ++j) lds_w128(xo + j * 1024, f[j]);
        *(LAS float*)(uintptr_t)(xo - lane * 12 + 4096) = lse;
        if (!G) {
#pragma unroll
            for (int j = 0; j < 4; ++j) { dmaO1(Wob + pcol(6) * 64, L0 + xslot(w4) + 8192, j); dmaO1(Wob + pcol(7) * 64, L0 + xslot(w4) + 12288, j); }
        }
        BARRIER();
        const u64 epoch = __hip_atomic_load((GAS u64*)(ws + WS_EPOCH + b * 256), RLX_AGENT);
        const float lseO = *(const LAS float*)(uintptr_t)(xi - lane * 12 + 4096);
#pragma unroll
        for (int j = 0; j < 4; ++j) f[j] = *(const LAS u32x4*)(uintptr_t)(xi + j * 1024);
        const float mx = __builtin_fmaxf(lse, lseO), ea = __builtin_amdgcn_exp2f(lse - mx), eb = __builtin_amdgcn_exp2f(lseO - mx);
        const float rden = __builtin_amdgcn_rcpf(ea + eb), ca = ea * rden * inv, cb = eb * rden, lseL = mx + __builtin_amdgcn_logf(ea + eb);
        __builtin_amdgcn_raw_buffer_store_b32(__float_as_uint(lseL), p1o, 4096 + lane * 4, 0, 16);
#pragma unroll
        for (int j = 0; j < 4; ++j) {
            u32x4 w;
#pragma unroll
            for (int e = 0; e < 4; ++e) {
                const int r = 8 * (j & 1) + 2 * e; f32x16& oo = o[2 * G + (j >> 1)];
                oo[r] = oo[r] * ca + bflo(f[j][e]) * cb; oo[r + 1] = oo[r + 1] * ca + bfhi(f[j][e]) * cb;
                w[e] = cvtpk(oo[r], oo[r + 1]);
            }
            st_sc1(p1o, j * 1024 + lane * 16, w);
        }
        SBAR(); VMCNT(0); SBAR();
        tag = ((u64)__builtin_amdgcn_readfirstlane((unsigned)(epoch >> 32)) << 32 | (unsigned)__builtin_amdgcn_readfirstlane((unsigned)epoch)) * 0x9E3779B97F4A7C15ull + 0x632BE59BD9B4E019ull;
        if (lane == 0) __hip_atomic_store((GAS u64*)(ws + WS_F1 + (me * 8 + wid) * 64), tag, RLX_AGENT);
        BARRIER();
        if (G) {
#pragma unroll
            for (int ks = 0; ks < 16; ++ks) {
                const unsigned a = L0 + (w4 < 2 ? (w4 ? VR + 3 * TILEB : KR) + ks * 1024 : xslot(ks >> 2) + w4 * 4096 + (ks & 3) * 1024) + wor;
                wf[ks] = cat(lds_tr(a), lds_tr(a + 512));
            }
        }
        wait_flag((GAS u64*)(ws + WS_F1 + ((me ^ 1) * 8 + wid) * 64), tag);
        const float lseR = __uint_as_float(__builtin_amdgcn_raw_buffer_load_b32(p1i, 4096 + lane * 4, 0, 16));
#pragma unroll
        for (int j = 0; j < 4; ++j) f[j] = ld_sc1(p1i, j * 1024 + lane * 16);
        const float mx2 = __builtin_fmaxf(lseL, lseR), e0 = __builtin_amdgcn_exp2f(lseL - mx2), e1 = __builtin_amdgcn_exp2f(lseR - mx2);
        const float rd2 = __builtin_amdgcn_rcpf(e0 + e1), c0 = e0 * rd2, c1 = e1 * rd2;
#pragma unroll
        for (int j = 0; j < 4; ++j) {
            u32x4 w;
#pragma unroll
            for (int e = 0; e < 4; ++e) {
                const int r = 8 * (j & 1) + 2 * e; const f32x16& oo = o[2 * G + (j >> 1)];
                w[e] = cvtpk(oo[r] * c0 + bflo(f[j][e]) * c1, oo[r + 1] * c0 + bfhi(f[j][e]) * c1);
            }
            lds_w128(xa + (4 * G + j) * 1024, w);
        }
        BARRIER();
    };
    if (g) TAIL(std::integral_constant<int, 1>{}); else TAIL(std::integral_constant<int, 0>{});
    {
        const __amdgpu_buffer_rsrc_t p2 = __builtin_amdgcn_make_buffer_rsrc(ws + WS_P2 + (size_t)(b * 2 + sp) * P2STRIDE, 0, 16 * 36864, 0x00020000);
        auto put = [&](const f32x16& c, unsigned piece) {
#pragma unroll
            for (int u = 0; u < 2; ++u) {
                u32x4 f;
#pragma unroll
                for (int e = 0; e < 4; ++e) f[e] = cvtpk(c[8 * u + 2 * e], c[8 * u + 2 * e + 1]);
                st_sc1(p2, kvh * 36864 + (piece + u) * 1024 + lane * 16, f);
            }
        };
        constexpr int LB = 6;
        const unsigned rbf = wid & 1;
        const unsigned ob[4] = {L0 + xslot(rbf) + lane * 16, L0 + xslot(2 + rbf) + lane * 16, L0 + xslot(rbf ^ 1) + lane * 16, L0 + xslot(2 + (rbf ^ 1)) + lane * 16};
        {
            bf16x8 f[32]; f32x16 c = {}, c2 = {};
            auto rd = [&](int i) { f[i] = lds_b128(ob[i >> 3] + (i & 7) * 1024); };
#pragma unroll
            for (int i = 0; i < LB; ++i) rd(i);
            SBAR();
#pragma unroll
            for (int i = 0; i < 32; ++i) {
                if (i + LB < 32) rd(i + LB);
                if (i < 16) c = MFMA(wf[i], f[i], c); else c2 = MFMA(wf[i - 16], f[i], c2);
                if (i == 19) put(c, wid * 4 + rbf * 2);
                SBAR();
            }
            if (wid < 2) {
                bf16x8 fa[16]; c = f32x16{};
                auto rd8 = [&](int i) { const unsigned a = L0 + KR + 1 * TILEB + wor + i * 1024; fa[i] = cat(lds_tr(a), lds_tr(a + 512)); f[i] = lds_b128(ob[i >> 3] + (i & 7) * 1024); };
#pragma unroll
                for (int i = 0; i < 4; ++i) rd8(i);
                SBAR();
#pragma unroll
                for (int i = 0; i < 16; ++i) {
                    if (i + 4 < 16) rd8(i + 4);
                    c = MFMA(fa[i], f[i], c);
                    if (i == 3) put(c2, wid * 4 + (rbf ^ 1) * 2);
                    SBAR();
                }
                put(c, 32 + wid * 2);
            } else put(c2, wid * 4 + (rbf ^ 1) * 2);
        }
        VMCNT(0);
        GAS u64* f2 = (GAS u64*)(ws + WS_F2);
        {
            const unsigned cons = lane < 2 ? wid * 2 + lane : wid * 8 + (lane - 2);
            if (lane < (wid < 2 ? 10u : 2u)) __hip_atomic_store(f2 + (size_t)((b * HKV + cons) * 2 + sp) * 32 + (lane < 2 ? 0 : 16) + kvh, tag, RLX_AGENT);
        }
        {
            if (wid == 7) wait_flag(f2 + (size_t)me * 32 + (lane & 31), tag);
            const unsigned sg = lane >> 4, c16 = lane & 15;
            const unsigned cc = kvh * 128 + wid * 16 + c16, c8 = 2048 + kvh * 16 + c16;
            auto oaddr = [&](unsigned c) {
                const unsigned piece = c >> 6, ln = c & 63, nb = piece >> 2, rb = (piece >> 1) & 1, u = piece & 1;
                return Out + ((size_t)(b * 64 + rb * 32 + (ln & 31)) * DM + sp * 288 + pcol(nb) * 32 + u * 16 + (ln >> 5) * 4 + (sg >> 1) * 8 + (sg & 1) * 2) * 2;
            };
            char* op = oaddr(cc); char* op8 = oaddr(c8);
            asm volatile("" : "+v"(op), "+v"(op8));
            BAR0();
            auto fin = [&](const u32x4* v, char* o_) {
                float a[8];
#pragma unroll
                for (int e = 0; e < 4; ++e) { a[2 * e] = (bflo(v[0][e]) + bflo(v[1][e])) + (bflo(v[2][e]) + bflo(v[3][e])); a[2 * e + 1] = (bfhi(v[0][e]) + bfhi(v[1][e])) + (bfhi(v[2][e]) + bfhi(v[3][e])); }
                float h[4], q[2];
#pragma unroll
                for (int e = 0; e < 4; ++e) { auto r = __builtin_amdgcn_permlane32_swap(__float_as_uint(a[e]), __float_as_uint(a[e + 4]), false, false); h[e] = __uint_as_float(r[0]) + __uint_as_float(r[1]); }
#pragma unroll
                for (int e = 0; e < 2; ++e) { auto r = __builtin_amdgcn_permlane16_swap(__float_as_uint(h[e]), __float_as_uint(h[e + 2]), false, false); q[e] = __uint_as_float(r[0]) + __uint_as_float(r[1]); }
                *(GAS unsigned*)o_ = cvtpk(q[0], q[1]);
            };
            u32x4 v[4], v8[4];
#pragma unroll
            for (int s = 0; s < 4; ++s) v[s] = ld_sc1(p2, (sg * 4 + s) * 36864 + cc * 16);
            if (wid == 6) {
#pragma unroll
                for (int s = 0; s < 4; ++s) v8[s] = ld_sc1(p2, (sg * 4 + s) * 36864 + c8 * 16);
            }
            if (wid == 0 && kvh == 0 && sp == 0 && lane == 0) __hip_atomic_store((GAS u64*)(ws + WS_EPOCH + b * 256), tag, RLX_AGENT);
            fin(v, op);
            if (wid == 6) fin(v8, op8);
            { const u64 te = __builtin_amdgcn_s_memrealtime() + MULT * (tm1 - tm0); while (__builtin_amdgcn_s_memrealtime() < te) __builtin_amdgcn_s_sleep(4); }
        }
    }
}

extern "C" void kernel_launch(void* const* d_in, const int* in_sizes, int n_in, void* d_out, int out_size, void* d_ws, size_t ws_size, hipStream_t stream) {
    (void)hipFuncSetAttribute((const void*)gqa_fused, hipFuncAttributeMaxDynamicSharedMemorySize, LDS_BYTES);
    hipLaunchKernelGGL(gqa_fused, dim3(256), dim3(512), LDS_BYTES, stream, (const char*)d_in[0], (const char*)d_in[1], (const char*)d_in[2], (const char*)d_in[3],
                       (const char*)d_in[4], (char*)d_out, (char*)d_ws);
}
```

```cpp
#include <hip/hip_runtime.h>
#include <stdint.h>
#include <type_traits>

#define DI __device__ __forceinline__
#define LAS __attribute__((address_space(3)))
#define GAS __attribute__((address_space(1)))
typedef __bf16 bf16x8 __attribute__((ext_vector_type(8)));
typedef __bf16 bf16x2 __attribute__((ext_vector_type(2)));
typedef short s16x4 __attribute__((ext_vector_type(4)));
typedef short s16x8 __attribute__((ext_vector_type(8)));
typedef float f32x2 __attribute__((ext_vector_type(2)));
typedef float f32x16 __attribute__((ext_vector_type(16)));
typedef unsigned u32x4 __attribute__((ext_vector_type(4)));
typedef unsigned u32x2 __attribute__((ext_vector_type(2)));
typedef unsigned long long u64;

constexpr int SQ = 64, DM = 576, HKV = 16, SK = 2048;
constexpr unsigned TILEB = 16384;
constexpr unsigned KR = 0, VR = 5 * TILEB;
constexpr unsigned SCR = 8 * TILEB;
constexpr unsigned LDS_BYTES = 10 * TILEB;
constexpr unsigned CODE_BYTES = 21120;
constexpr float QSCALE = 0.08838834764831845f * 1.4426950408889634f;

#define AI __attribute__((always_inline))
#define SBAR() __builtin_amdgcn_sched_barrier(0)
#define MFMA(a, b, c) __builtin_amdgcn_mfma_f32_32x32x16_bf16((a), (b), (c), 0, 0, 0)
#define VMCNT(n) asm volatile("s_waitcnt vmcnt(%0)" :: "i"(n) : "memory")
#define BARRIER() asm volatile("s_waitcnt lgkmcnt(0)\n\ts_barrier" ::: "memory")
#define BAR0() asm volatile("s_barrier" ::: "memory")
#define RLX_AGENT __ATOMIC_RELAXED, __HIP_MEMORY_SCOPE_AGENT

DI unsigned cvtpk(float lo, float hi) { f32x2 v = {lo, hi}; bf16x2 b = __builtin_convertvector(v, bf16x2); return __builtin_bit_cast(unsigned, b); }
DI float bflo(unsigned u) { return __uint_as_float(u << 16); }
DI float bfhi(unsigned u) { return __uint_as_float(u & 0xffff0000u); }
DI bf16x8 lds_b128(unsigned a) { return *(const LAS bf16x8*)(uintptr_t)a; }
DI s16x4 lds_tr(unsigned a) { return __builtin_amdgcn_ds_read_tr16_b64_v4i16((LAS s16x4*)(uintptr_t)a); }
DI bf16x8 cat(s16x4 lo, s16x4 hi) { s16x8 r = __builtin_shufflevector(lo, hi, 0, 1, 2, 3, 4, 5, 6, 7); return __builtin_bit_cast(bf16x8, r); }
DI void lds_w128(unsigned a, u32x4 v) { *(LAS u32x4*)(uintptr_t)a = v; }
template <int NT, int OFF> DI void dma1(const void* sbase, unsigned voff, unsigned m) {
    if constexpr (NT) asm volatile("s_mov_b32 m0, %2\n\ts_nop 0\n\tglobal_load_lds_dwordx4 %0, %1 offset:%3 nt" :: "v"(voff), "s"(sbase), "s"(m), "i"(OFF) : "memory");
    else asm volatile("s_mov_b32 m0, %2\n\ts_nop 0\n\tglobal_load_lds_dwordx4 %0, %1 offset:%3" :: "v"(voff), "s"(sbase), "s"(m), "i"(OFF) : "memory");
}
template <int NT, int OFF> DI void dma2(const void* sbase, unsigned v0, unsigned v1, unsigned m) {
    if constexpr (NT) asm volatile("s_mov_b32 m0, %3\n\ts_nop 0\n\tglobal_load_lds_dwordx4 %0, %2 offset:%4 nt\n\tglobal_load_lds_dwordx4 %1, %2 offset:%5 nt" :: "v"(v0), "v"(v1), "s"(sbase), "s"(m), "i"(OFF), "i"(OFF + 1024) : "memory");
    else asm volatile("s_mov_b32 m0, %3\n\ts_nop 0\n\tglobal_load_lds_dwordx4 %0, %2 offset:%4\n\tglobal_load_lds_dwordx4 %1, %2 offset:%5" :: "v"(v0), "v"(v1), "s"(sbase), "s"(m), "i"(OFF), "i"(OFF + 1024) : "memory");
}
DI f32x16 mfma_c(bf16x8 a, bf16x8 b, const f32x16& c) {
    f32x16 d; asm("s_nop 1\n\tv_mfma_f32_32x32x16_bf16 %0, %1, %2, %3" : "=&v"(d) : "v"(a), "v"(b), "v"(c)); return d; }
DI float fsub_s(float a, float b) { float r; asm("v_sub_f32_e32 %0, %1, %2" : "=v"(r) : "v"(a), "v"(b)); return r; }
DI float swapmax(float x) { auto r = __builtin_amdgcn_permlane32_swap(__float_as_uint(x), __float_as_uint(x), false, false); return __builtin_fmaxf(__uint_as_float(r[0]), __uint_as_float(r[1])); }
DI float swapadd(float x) { auto r = __builtin_amdgcn_permlane32_swap(__float_as_uint(x), __float_as_uint(x), false, false); return __uint_as_float(r[0]) + __uint_as_float(r[1]); }
#define MX3(a, b, c) __builtin_fmaxf(__builtin_fmaxf((a), (b)), (c))
DI void st_sc1(__amdgpu_buffer_rsrc_t r, unsigned off, u32x4 v) { __builtin_amdgcn_raw_buffer_store_b128(v, r, off, 0, 16); }
DI u32x4 ld_sc1(__amdgpu_buffer_rsrc_t r, unsigned off) { return __builtin_amdgcn_raw_buffer_load_b128(r, off, 0, 16); }
DI void wait_flag(GAS u64* f, u64 tag) {
    u64 v0 = __hip_atomic_load(f, RLX_AGENT);
    if (!__all(v0 == tag)) {
        v0 = __hip_atomic_load(f, RLX_AGENT);
        __builtin_amdgcn_s_sleep(8);
#pragma unroll 1
        for (unsigned spin = 0; spin < (1u << 20); ++spin) {
            const u64 v1 = __hip_atomic_load(f, RLX_AGENT);
            if (__all(v0 == tag)) break;
            v0 = __hip_atomic_load(f, RLX_AGENT);
            if (__all(v1 == tag)) break;
        }
    }
    asm volatile("" ::: "memory");
}

DI unsigned pace(unsigned tn, unsigned per) {
    unsigned nx;
    asm volatile("1:\n\ts_memrealtime s[98:99]\n\ts_waitcnt lgkmcnt(0)\n\ts_sub_i32 s99, s98, %1\n\ts_cmp_lt_i32 s99, 0\n\ts_cbranch_scc1 1b\n\ts_add_i32 %0, s98, %2"
                 : "=s"(nx) : "s"(tn), "s"(per) : "memory", "scc", "s98", "s99");
    return nx;
}

constexpr size_t WS_EPOCH = 0;
constexpr size_t WS_F1 = 65536;
constexpr size_t WS_F2 = 262144;
constexpr size_t WS_P1 = 1u << 20;
constexpr size_t WS_P2 = 12u << 20;
constexpr unsigned P2STRIDE = 16 * 36864;

using T_ = std::integral_constant<bool, true>; using F_ = std::integral_constant<bool, false>;

__global__ __launch_bounds__(512, 2) void gqa_fused(const char* X, const char* Kc, const char* Vc, const char* Wq, const char* Wo, char* Out, char* ws) {
    extern __shared__ __attribute__((aligned(16))) char smem[];
    asm volatile("" : "+s"(X), "+s"(Kc), "+s"(Vc), "+s"(Wq), "+s"(Wo), "+s"(Out), "+s"(ws));
    u64 pc0; asm volatile("s_getpc_b64 %0" : "=s"(pc0));
    const unsigned tid = threadIdx.x, lane = tid & 63;
    const unsigned wid = __builtin_amdgcn_readfirstlane(tid >> 6);
    const unsigned r32 = lane & 31, hi = lane >> 5, w4 = wid & 3, g = wid >> 2;
    const unsigned L0 = (unsigned)(uintptr_t)smem;
    const unsigned wg = blockIdx.x, xcd = wg & 7, slot = wg >> 3;
    const unsigned kvh = (xcd & 3) * 4 + (slot & 3), sp = xcd >> 2, b = slot >> 2;
    const unsigned pair = b * HKV + kvh, me = pair * 2 + sp;
    constexpr unsigned TS = 16384;
    const unsigned woff = sp * 262144 + w4 * 4096;
    const char* Kb = Kc + (size_t)pair * SK * 256 + woff;
    const char* Vb = Vc + (size_t)pair * SK * 256 + woff;
    const unsigned kv0 = (lane >> 4) * 256 + (((lane & 15) ^ (lane >> 4)) << 4), kv1 = kv0 ^ 64, kv2 = kv0 ^ 128, kv3 = kv0 ^ 192;
    const unsigned vvoff = (lane >> 4) * 256 + (((lane & 15) ^ ((lane >> 4) << 2)) << 4);
    const unsigned Lw = L0 + w4 * 1024, Lq = L0 + w4 * 4096;
    const unsigned rot = b * 2;
    auto dmaK = [&](unsigned t, unsigned sl) { t = (t + rot) & 15; dma2<1, 0>(Kb + t * TS, kv0, kv1, Lq + KR + sl * TILEB); dma2<1, 2048>(Kb + t * TS, kv2, kv3, Lq + KR + sl * TILEB); };
    auto dmaV = [&](unsigned t, unsigned sl) { t = (t + rot) & 15; dma2<1, 0>(Vb + t * TS, vvoff, vvoff, Lq + VR + sl * TILEB); dma2<1, 2048>(Vb + t * TS, vvoff, vvoff, Lq + VR + sl * TILEB); };
    const unsigned g1 = (lane >> 4) & 1, q4 = (lane & 15) >> 2, p4 = lane & 3;

    bf16x8 qf[8];
    auto touch = [&](const void* sbase, unsigned voff, unsigned dst) {
        asm volatile("s_mov_b32 m0, %2\n\ts_nop 0\n\tglobal_load_lds_dword %0, %1" :: "v"(voff), "s"(sbase), "s"(dst) : "memory");
    };
    {
        constexpr unsigned XR = 3 * TILEB, WR = 6 * TILEB;
        if (g) {
            const char* Xb = X + (size_t)b * SQ * DM * 2;
            const unsigned xvoff = (8 * w4 + (lane >> 3)) * (DM * 2) + (((lane & 7) ^ (4 * (w4 & 1) + (lane >> 4))) << 4);
            const char* Wb = Wq + (2 * kvh + (w4 >> 1)) * 256 + (size_t)(w4 & 1) * 16 * 8192;
            const unsigned wvoff = (lane >> 4) * 8192 + (((lane & 15) ^ ((lane >> 4) << 2)) << 4);
            unsigned cb = b + (kvh & 3) * 2; cb = cb >= 9 ? cb - 9 : cb;
            unsigned xb = cb;
            auto xhalf = [&](unsigned j, unsigned h) { dma1<0, 0>(Xb + xb * 128 + h * (32 * DM * 2), xvoff, Lw + XR + (j & 3) * 8192 + h * 4096); if (h) xb = xb == 8 ? 0 : xb + 1; };
            auto phase = [&](unsigned p) {
                xhalf((p + 1) >> 1, (p & 1) ^ 1);
                const char* src = Wb + (size_t)(cb * 2 + (p & 1)) * (32 * 8192);
                dma2<0, 0>(src, wvoff, wvoff + 31744, Lq + WR + (p & 3) * TILEB); dma2<0, 2048>(src, wvoff + 2 * 31744, wvoff + 3 * 31744, Lq + WR + (p & 3) * TILEB);
                if (p & 1) cb = cb == 8 ? 0 : cb + 1;
            };
            for (unsigned i = 0; i < (b & 3) * 4; ++i) __builtin_amdgcn_s_sleep(4);
            xhalf(0, 0);
            phase(0); phase(1); phase(2);
            VMCNT(10); BAR0();
            phase(3);
#pragma unroll 1
            for (unsigned p = 4; p < 18; ++p) { VMCNT(10); BAR0(); phase(p); }
            VMCNT(10); BAR0(); dmaK(0, 0);
            VMCNT(9); BAR0(); dmaK(1, 1);
            VMCNT(8); BAR0(); dmaK(2, 2);
            BAR0();
            dmaV(0, 0);
            VMCNT(12); BAR0();
#pragma unroll
            for (int s = 0; s < 8; ++s) qf[s] = lds_b128(L0 + SCR + (w4 * 8 + s) * 1024 + lane * 16);
            SBAR();
            dmaK(3, 3); dmaV(1, 1);
            VMCNT(16);
        } else {
            if (w4 == 0) { const unsigned ln = slot * 8 + (lane & 7); touch((const void*)(pc0 & ~127ull), ln * 128 < CODE_BYTES ? ln * 128 : 0u, Lw + VR); }
            const unsigned ps = ((p4 & 1) << 1) | (p4 >> 1);
            const unsigned wa0 = (L0 + WR + (w4 >> 1) * 8192 + (8 * hi + q4) * 256 + (q4 << 6) + g1 * 32 + ps * 8) ^ ((w4 & 1) << 7);
            const unsigned xa0 = L0 + XR + r32 * 128 + ((hi ^ ((r32 >> 1) & 7)) << 4);
            f32x16 acc[4] = {};
            bf16x8 fa[2][4], fb[2][4];
            auto rdA = [&](int set, unsigned p, int i) { const unsigned a = ((wa0 + (p & 3) * TILEB) ^ ((i & 1) << 6)) + (i >> 1) * 4096; fa[set][i] = cat(lds_tr(a), lds_tr(a + 1024)); };
            auto rdB = [&](int set, unsigned p, int i) { fb[set][i] = lds_b128(((xa0 + ((p >> 1) & 3) * 8192) ^ ((2 * set + (i >> 1)) << 5)) + (i & 1) * 4096); };
            auto step = [&](auto set_, auto last_, unsigned pn) AI {
                constexpr int set = decltype(set_)::value; constexpr bool last = decltype(last_)::value;
                SBAR(); BARRIER(); SBAR();
                __builtin_amdgcn_s_sleep(2);
#pragma unroll
                for (int i = 0; i < 8; ++i) {
                    acc[i & 3] = MFMA(fa[set][2 * (i >> 2) + ((i >> 1) & 1)], fb[set][2 * (i >> 2) + (i & 1)], acc[i & 3]);
                    if (!last && i < 4) { rdA(set ^ 1, pn, i); rdB(set ^ 1, pn, i); }
                    SBAR();
                }
            };
            using I0 = std::integral_constant<int, 0>; using I1 = std::integral_constant<int, 1>;
            BAR0();
#pragma unroll
            for (int i = 0; i < 4; ++i) { rdA(0, 0, i); rdB(0, 0, i); }
#pragma unroll 1
            for (unsigned p = 0; p < 16; p += 2) { if (p == 12) VMCNT(0); step(I0{}, F_{}, p + 1); step(I1{}, F_{}, p + 2); }
            step(I0{}, F_{}, 17); step(I1{}, T_{}, 0);
#pragma unroll
            for (int s = 0; s < 8; ++s) {
                u32x4 f;
#pragma unroll
                for (int e = 0; e < 4; ++e) f[e] = cvtpk(acc[s >> 1][8 * (s & 1) + 2 * e] * QSCALE, acc[s >> 1][8 * (s & 1) + 2 * e + 1] * QSCALE);
                lds_w128(L0 + SCR + (((w4 & 2) + ((s >> 1) & 1)) * 8 + (w4 & 1) * 4 + (s >> 2) * 2 + (s & 1)) * 1024 + lane * 16, f);
            }
            BARRIER();
#pragma unroll
            for (int s = 0; s < 8; ++s) qf[s] = lds_b128(L0 + SCR + (w4 * 8 + s) * 1024 + lane * 16);
        }
    }

    const unsigned kr0 = L0 + KR + r32 * 256 + ((hi ^ (r32 & 15)) << 4);
    const unsigned vr0 = L0 + VR + (4 * hi + q4) * 256 + (q4 << 6) + g1 * 32 + p4 * 8;
    const char* Wob = Wo + ((size_t)(kvh * 256 + w4 * 64) * DM + sp * 288) * 2;
    const unsigned ov0 = (lane >> 2) * (DM * 2) + (lane & 3) * 16;
    constexpr unsigned OVS = 16 * DM * 2 - 1024;
    auto pcol = [&](unsigned nb) { return sp ? (nb == 8 ? 0u : nb + 1) : nb; };
    auto dmaO1 = [&](const char* src, unsigned m, int j) {
        if (j == 0) dma1<0, 0>(src, ov0, m); else if (j == 1) dma1<0, 1024>(src, ov0 + OVS, m); else if (j == 2) dma1<0, 2048>(src, ov0 + 2 * OVS, m); else dma1<0, 3072>(src, ov0 + 3 * OVS, m);
    };
    f32x16 o[4] = {};
    f32x16 s0, s1, negm = {};
    u32x4 pf[4];
    float m = 0.f, l = 0.f;

    constexpr int LA = 6;
    auto rdK = [&](unsigned ka, int i) { return lds_b128((ka ^ ((i >> 1) << 5)) + (i & 1) * 8192); };
    auto rdV = [&](unsigned va, int i) { const unsigned a = (va ^ ((i & 3) << 6)) + (i >> 2) * 4096; return cat(lds_tr(a), lds_tr(a + 2048)); };
    auto MPH = [&](auto doqk_, auto dopv_, unsigned ksl_, unsigned vsl_) AI {
        constexpr bool DQ = decltype(doqk_)::value, DP = decltype(dopv_)::value;
        constexpr int N = 16 * (int)DQ + 16 * (int)DP, OFFV = DQ ? 16 : 0;
        const unsigned ka = kr0 + ksl_ * TILEB, va = vr0 + vsl_ * TILEB;
        bf16x8 f[N];
        auto rd = [&](int i) { if (DQ && i < 16) f[i] = rdK(ka, i); else f[i] = rdV(va, i - OFFV); };
        __builtin_amdgcn_s_setprio(1);
#pragma unroll
        for (int i = 0; i < LA; ++i) rd(i);
        SBAR();
#pragma unroll
        for (int i = 0; i < N; ++i) {
            if (i + LA < N) rd(i + LA);
            if (DQ && i < 16) {
                if (i == 0) s0 = mfma_c(f[i], qf[0], negm); else if (i == 1) s1 = mfma_c(f[i], qf[0], negm);
                else if (i & 1) s1 = MFMA(f[i], qf[i >> 1], s1); else s0 = MFMA(f[i], qf[i >> 1], s0);
            } else { const int j = i - OFFV; o[j & 3] = MFMA(f[i], __builtin_bit_cast(bf16x8, pf[j >> 2]), o[j & 3]); }
            SBAR();
        }
        __builtin_amdgcn_s_setprio(0);
    };
    auto RESCALE = [&](auto first_) AI {
        constexpr bool first = decltype(first_)::value;
        float a = MX3(s0[0], s0[1], s1[0]), c = MX3(s0[2], s0[3], s1[1]); a = MX3(a, s1[2], s1[3]);
#pragma unroll
        for (int r = 4; r < 16; r += 4) { a = MX3(a, s0[r], s0[r + 1]); c = MX3(c, s0[r + 2], s0[r + 3]); a = MX3(a, s1[r], s1[r + 1]); c = MX3(c, s1[r + 2], s1[r + 3]); }
        const float rm = swapmax(__builtin_fmaxf(a, c));
        const float dl = first ? rm : __builtin_fmaxf(rm, 0.f);
        m += dl;
#pragma unroll
        for (int r = 0; r < 16; ++r) { s0[r] = fsub_s(s0[r], dl); s1[r] = fsub_s(s1[r], dl); negm[r] = fsub_s(negm[r], dl); }
        if (!first) {
            const float f = __builtin_amdgcn_exp2f(-dl);
            l *= f;
#pragma unroll
            for (int db = 0; db < 4; ++db) o[db] *= f;
        }
    };
    auto EXPS = [&](auto&& hook) AI {
        float sa = 0.f, sb = 0.f;
#pragma unroll
        for (int k = 0; k < 8; ++k) {
            const f32x16& s = k < 4 ? s0 : s1; const int r0 = (k & 3) * 4;
            const float e0 = __builtin_amdgcn_exp2f(s[r0]), e1 = __builtin_amdgcn_exp2f(s[r0 + 1]), e2 = __builtin_amdgcn_exp2f(s[r0 + 2]), e3 = __builtin_amdgcn_exp2f(s[r0 + 3]);
            if (k == 0) { sa = e0 + e2; sb = e1 + e3; } else { sa += e0; sb += e1; sa += e2; sb += e3; }
            unsigned w0 = cvtpk(e0, e1), w1 = cvtpk(e2, e3);
            asm volatile("" : "+v"(w0), "+v"(w1), "+v"(sa), "+v"(sb));
            pf[k >> 1][(k & 1) * 2] = w0; pf[k >> 1][(k & 1) * 2 + 1] = w1;
            SBAR(); hook(k); SBAR();
        }
        return sa + sb;
    };
    auto SM = [&](auto first_, auto&& hook) AI {
        constexpr bool first = decltype(first_)::value;
        if (first) RESCALE(first_);
        float ts = EXPS(hook);
        if (!first && __builtin_expect(__any(!(ts <= 256.f)), 0)) { RESCALE(F_{}); ts = EXPS([](int) {}); }
        l += ts;
        asm volatile("" : "+v"(l));
    };
    auto inc2 = [](unsigned s) { return s >= 3 ? s - 3 : s + 2; };
    unsigned t = g;
    unsigned kq = g, vq = g;
    const char* ksrc; const char* vsrc;
    unsigned mK, mV;
    auto hookKV = [&](int j) {
        if (j == 0) dma2<1, 0>(ksrc, kv0, kv1, mK); else if (j == 2) dma2<1, 2048>(ksrc, kv2, kv3, mK);
        else if (j == 4) dma2<1, 0>(vsrc, vvoff, vvoff, mV); else if (j == 6) dma2<1, 2048>(vsrc, vvoff, vvoff, mV);
    };
    auto prep = [&]() { ksrc = Kb + ((t + 4 + rot) & 15) * TS; vsrc = Vb + ((t + 2 + rot) & 15) * TS; mK = Lq + KR + (kq ? kq - 1 : 4) * TILEB; mV = Lq + VR + inc2(vq) * TILEB; asm volatile("" : "+s"(mK), "+s"(mV), "+s"(ksrc), "+s"(vsrc)); };
    unsigned tnext = (unsigned)__builtin_amdgcn_s_memrealtime();
    asm volatile("" : "+s"(tnext));
    if (g) { SBAR(); BARRIER(); SBAR(); }
    prep();
    MPH(T_{}, F_{}, kq, 0);
    SBAR();
    if (g) VMCNT(8);
    BARRIER(); SBAR();
    SM(T_{}, hookKV); SBAR();
    VMCNT(8);
#pragma unroll 1
    while (t <= 9) {
        const unsigned vcur = vq;
        kq = inc2(kq); vq = inc2(vq); t += 2; prep();
        SBAR(); BARRIER(); SBAR();
        MPH(T_{}, T_{}, kq, vcur);
        SBAR(); BARRIER(); SBAR();
        tnext = pace(tnext, 240); SBAR();
        SM(F_{}, hookKV); SBAR();
        VMCNT(8);
    }
#pragma unroll 1
    while (t <= 13) {
        const unsigned vcur = vq;
        kq = inc2(kq); vq = inc2(vq); t += 2; prep();
        const char* oA = Wob + pcol(t == 12 ? 8 : 2 * t - 26) * 64; const char* oB = Wob + pcol(2 * t - 25) * 64;
        unsigned mA = Lq + KR + (t == 12 ? 1 : kq) * TILEB, mB = Lq + VR + (t - 12) * TILEB;
        asm volatile("" : "+s"(oA), "+s"(oB), "+s"(mA), "+s"(mB));
        SBAR(); BARRIER(); SBAR();
        MPH(T_{}, T_{}, kq, vcur);
        SBAR(); BARRIER(); SBAR();
        if (t <= 13) { hookKV(4); hookKV(6); }
        SBAR();
        SM(F_{}, [&](int j) { if (!(j & 1)) { dmaO1(oA, mA, j >> 1); if (t >= 13) dmaO1(oB, mB, j >> 1); } }); SBAR();
        if (t == 13 || t == 14) VMCNT(12); else VMCNT(8);
    }
    SBAR(); BARRIER(); SBAR();
    MPH(F_{}, T_{}, 0, vq);
    l = swapadd(l);

    const unsigned wor = (4 * hi + q4) * 64 + g1 * 32 + p4 * 8;
    bf16x8 wf[16];
    auto xslot = [](unsigned w) { return (w & 1) ? VR + (1 + (w >> 1)) * TILEB : KR + (3 + (w >> 1)) * TILEB; };
    u64 tag;
    auto TAIL = [&](auto G_) AI {
        constexpr int G = decltype(G_)::value;
        const __amdgpu_buffer_rsrc_t p1o = __builtin_amdgcn_make_buffer_rsrc(ws + WS_P1 + (size_t)(me * 8 + wid) * 4608, 0, 4608, 0x00020000);
        const __amdgpu_buffer_rsrc_t p1i = __builtin_amdgcn_make_buffer_rsrc(ws + WS_P1 + (size_t)((me ^ 1) * 8 + wid) * 4608, 0, 4608, 0x00020000);
        const unsigned xa = L0 + xslot(w4) + lane * 16, xb = L0 + ((w4 & 2) ? VR + 4 * TILEB : KR + 2 * TILEB) + (w4 & 1) * 8192 + lane * 16;
        const float inv = __builtin_amdgcn_rcpf(l);
        float lse = m + __builtin_amdgcn_logf(l);
        u32x4 f[4];
#pragma unroll
        for (int j = 0; j < 4; ++j) {
#pragma unroll
            for (int e = 0; e < 4; ++e) { const int r = 8 * (j & 1) + 2 * e; const f32x16& oo = o[2 * (G ^ 1) + (j >> 1)]; f[j][e] = cvtpk(oo[r] * inv, oo[r + 1] * inv); }
            asm volatile("" : "+v"(f[j]));
        }
        if (!G) {
            asm volatile("" : "+v"(lse));
            SBAR(); VMCNT(0);
            BARRIER(); SBAR();
#pragma unroll
            for (int ks = 0; ks < 16; ++ks) wf[ks] = cat(lds_tr(L0 + xslot(w4) + wor + ks * 1024), lds_tr(L0 + xslot(w4) + wor + ks * 1024 + 512));
            asm volatile("s_waitcnt lgkmcnt(0)" ::: "memory");
        }
        const unsigned xo = G ? xb : xa, xi = G ? xa : xb;
#pragma unroll
        for (int j = 0; j < 4; ++j) lds_w128(xo + j * 1024, f[j]);
        *(LAS float*)(uintptr_t)(xo - lane * 12 + 4096) = lse;
        if (!G) {
#pragma unroll
            for (int j = 0; j < 4; ++j) { dmaO1(Wob + pcol(6) * 64, L0 + xslot(w4) + 8192, j); dmaO1(Wob + pcol(7) * 64, L0 + xslot(w4) + 12288, j); }
        }
        BARRIER();
        const u64 epoch = __hip_atomic_load((GAS u64*)(ws + WS_EPOCH + b * 256), RLX_AGENT);
        const float lseO = *(const LAS float*)(uintptr_t)(xi - lane * 12 + 4096);
#pragma unroll
        for (int j = 0; j < 4; ++j) f[j] = *(const LAS u32x4*)(uintptr_t)(xi + j * 1024);
        const float mx = __builtin_fmaxf(lse, lseO), ea = __builtin_amdgcn_exp2f(lse - mx), eb = __builtin_amdgcn_exp2f(lseO - mx);
        const float rden = __builtin_amdgcn_rcpf(ea + eb), ca = ea * rden * inv, cb = eb * rden, lseL = mx + __builtin_amdgcn_logf(ea + eb);
        __builtin_amdgcn_raw_buffer_store_b32(__float_as_uint(lseL), p1o, 4096 + lane * 4, 0, 16);
#pragma unroll
        for (int j = 0; j < 4; ++j) {
            u32x4 w;
#pragma unroll
            for (int e = 0; e < 4; ++e) {
                const int r = 8 * (j & 1) + 2 * e; f32x16& oo = o[2 * G + (j >> 1)];
                oo[r] = oo[r] * ca + bflo(f[j][e]) * cb; oo[r + 1] = oo[r + 1] * ca + bfhi(f[j][e]) * cb;
                w[e] = cvtpk(oo[r], oo[r + 1]);
            }
            st_sc1(p1o, j * 1024 + lane * 16, w);
        }
        SBAR(); VMCNT(0); SBAR();
        tag = ((u64)__builtin_amdgcn_readfirstlane((unsigned)(epoch >> 32)) << 32 | (unsigned)__builtin_amdgcn_readfirstlane((unsigned)epoch)) * 0x9E3779B97F4A7C15ull + 0x632BE59BD9B4E019ull;
        if (lane == 0) __hip_atomic_store((GAS u64*)(ws + WS_F1 + (me * 8 + wid) * 64), tag, RLX_AGENT);
        BARRIER();
        if (G) {
#pragma unroll
            for (int ks = 0; ks < 16; ++ks) {
                const unsigned a = L0 + (w4 < 2 ? (w4 ? VR + 3 * TILEB : KR) + ks * 1024 : xslot(ks >> 2) + w4 * 4096 + (ks & 3) * 1024) + wor;
                wf[ks] = cat(lds_tr(a), lds_tr(a + 512));
            }
        }
        wait_flag((GAS u64*)(ws + WS_F1 + ((me ^ 1) * 8 + wid) * 64), tag);
        const float lseR = __uint_as_float(__builtin_amdgcn_raw_buffer_load_b32(p1i, 4096 + lane * 4, 0, 16));
#pragma unroll
        for (int j = 0; j < 4; ++j) f[j] = ld_sc1(p1i, j * 1024 + lane * 16);
        const float mx2 = __builtin_fmaxf(lseL, lseR), e0 = __builtin_amdgcn_exp2f(lseL - mx2), e1 = __builtin_amdgcn_exp2f(lseR - mx2);
        const float rd2 = __builtin_amdgcn_rcpf(e0 + e1), c0 = e0 * rd2, c1 = e1 * rd2;
#pragma unroll
        for (int j = 0; j < 4; ++j) {
            u32x4 w;
#pragma unroll
            for (int e = 0; e < 4; ++e) {
                const int r = 8 * (j & 1) + 2 * e; const f32x16& oo = o[2 * G + (j >> 1)];
                w[e] = cvtpk(oo[r] * c0 + bflo(f[j][e]) * c1, oo[r + 1] * c0 + bfhi(f[j][e]) * c1);
            }
            lds_w128(xa + (4 * G + j) * 1024, w);
        }
        BARRIER();
    };
    if (g) TAIL(std::integral_constant<int, 1>{}); else TAIL(std::integral_constant<int, 0>{});
    {
        const __amdgpu_buffer_rsrc_t p2 = __builtin_amdgcn_make_buffer_rsrc(ws + WS_P2 + (size_t)(b * 2 + sp) * P2STRIDE, 0, 16 * 36864, 0x00020000);
        auto put = [&](const f32x16& c, unsigned piece) {
#pragma unroll
            for (int u = 0; u < 2; ++u) {
                u32x4 f;
#pragma unroll
                for (int e = 0; e < 4; ++e) f[e] = cvtpk(c[8 * u + 2 * e], c[8 * u + 2 * e + 1]);
                st_sc1(p2, kvh * 36864 + (piece + u) * 1024 + lane * 16, f);
            }
        };
        constexpr int LB = 6;
        const unsigned rbf = wid & 1;
        const unsigned ob[4] = {L0 + xslot(rbf) + lane * 16, L0 + xslot(2 + rbf) + lane * 16, L0 + xslot(rbf ^ 1) + lane * 16, L0 + xslot(2 + (rbf ^ 1)) + lane * 16};
        {
            bf16x8 f[32]; f32x16 c = {}, c2 = {};
            auto rd = [&](int i) { f[i] = lds_b128(ob[i >> 3] + (i & 7) * 1024); };
#pragma unroll
            for (int i = 0; i < LB; ++i) rd(i);
            SBAR();
#pragma unroll
            for (int i = 0; i < 32; ++i) {
                if (i + LB < 32) rd(i + LB);
                if (i < 16) c = MFMA(wf[i], f[i], c); else c2 = MFMA(wf[i - 16], f[i], c2);
                if (i == 19) put(c, wid * 4 + rbf * 2);
                SBAR();
            }
            if (wid < 2) {
                bf16x8 fa[16]; c = f32x16{};
                auto rd8 = [&](int i) { const unsigned a = L0 + KR + 1 * TILEB + wor + i * 1024; fa[i] = cat(lds_tr(a), lds_tr(a + 512)); f[i] = lds_b128(ob[i >> 3] + (i & 7) * 1024); };
#pragma unroll
                for (int i = 0; i < 4; ++i) rd8(i);
                SBAR();
#pragma unroll
                for (int i = 0; i < 16; ++i) {
                    if (i + 4 < 16) rd8(i + 4);
                    c = MFMA(fa[i], f[i], c);
                    if (i == 3) put(c2, wid * 4 + (rbf ^ 1) * 2);
                    SBAR();
                }
                put(c, 32 + wid * 2);
            } else put(c2, wid * 4 + (rbf ^ 1) * 2);
        }
        VMCNT(0);
        GAS u64* f2 = (GAS u64*)(ws + WS_F2);
        {
            const unsigned cons = lane < 2 ? wid * 2 + lane : wid * 8 + (lane - 2);
            if (lane < (wid < 2 ? 10u : 2u)) __hip_atomic_store(f2 + (size_t)((b * HKV + cons) * 2 + sp) * 32 + (lane < 2 ? 0 : 16) + kvh, tag, RLX_AGENT);
        }
        {
            if (wid == 7) wait_flag(f2 + (size_t)me * 32 + (lane & 31), tag);
            const unsigned sg = lane >> 4, c16 = lane & 15;
            const unsigned cc = kvh * 128 + wid * 16 + c16, c8 = 2048 + kvh * 16 + c16;
            auto oaddr = [&](unsigned c) {
                const unsigned piece = c >> 6, ln = c & 63, nb = piece >> 2, rb = (piece >> 1) & 1, u = piece & 1;
                return Out + ((size_t)(b * 64 + rb * 32 + (ln & 31)) * DM + sp * 288 + pcol(nb) * 32 + u * 16 + (ln >> 5) * 4 + (sg >> 1) * 8 + (sg & 1) * 2) * 2;
            };
            char* op = oaddr(cc); char* op8 = oaddr(c8);
            asm volatile("" : "+v"(op), "+v"(op8));
            BAR0();
            auto fin = [&](const u32x4* v, char* o_) {
                float a[8];
#pragma unroll
                for (int e = 0; e < 4; ++e) { a[2 * e] = (bflo(v[0][e]) + bflo(v[1][e])) + (bflo(v[2][e]) + bflo(v[3][e])); a[2 * e + 1] = (bfhi(v[0][e]) + bfhi(v[1][e])) + (bfhi(v[2][e]) + bfhi(v[3][e])); }
                float h[4], q[2];
#pragma unroll
                for (int e = 0; e < 4; ++e) { auto r = __builtin_amdgcn_permlane32_swap(__float_as_uint(a[e]), __float_as_uint(a[e + 4]), false, false); h[e] = __uint_as_float(r[0]) + __uint_as_float(r[1]); }
#pragma unroll
                for (int e = 0; e < 2; ++e) { auto r = __builtin_amdgcn_permlane16_swap(__float_as_uint(h[e]), __float_as_uint(h[e + 2]), false, false); q[e] = __uint_as_float(r[0]) + __uint_as_float(r[1]); }
                *(GAS unsigned*)o_ = cvtpk(q[0], q[1]);
            };
            u32x4 v[4], v8[4];
#pragma unroll
            for (int s = 0; s < 4; ++s) v[s] = ld_sc1(p2, (sg * 4 + s) * 36864 + cc * 16);
            if (wid == 6) {
#pragma unroll
                for (int s = 0; s < 4; ++s) v8[s] = ld_sc1(p2, (sg * 4 + s) * 36864 + c8 * 16);
            }
            if (wid == 0 && kvh == 0 && sp == 0 && lane == 0) __hip_atomic_store((GAS u64*)(ws + WS_EPOCH + b * 256), tag, RLX_AGENT);
            fin(v, op);
            if (wid == 6) fin(v8, op8);
        }
    }
}

extern "C" void kernel_launch(void* const* d_in, const int* in_sizes, int n_in, void* d_out, int out_size, void* d_ws, size_t ws_size, hipStream_t stream) {
    (void)hipFuncSetAttribute((const void*)gqa_fused, hipFuncAttributeMaxDynamicSharedMemorySize, LDS_BYTES);
    hipLaunchKernelGGL(gqa_fused, dim3(256), dim3(512), LDS_BYTES, stream, (const char*)d_in[0], (const char*)d_in[1], (const char*)d_in[2], (const char*)d_in[3],
                       (const char*)d_in[4], (char*)d_out, (char*)d_ws);
}
```

```cpp
#include <hip/hip_runtime.h>
#include <stdint.h>
#include <type_traits>

#define DI __device__ __forceinline__
#define LAS __attribute__((address_space(3)))
#define GAS __attribute__((address_space(1)))
typedef __bf16 bf16x8 __attribute__((ext_vector_type(8)));
typedef __bf16 bf16x2 __attribute__((ext_vector_type(2)));
typedef short s16x4 __attribute__((ext_vector_type(4)));
typedef short s16x8 __attribute__((ext_vector_type(8)));
typedef float f32x2 __attribute__((ext_vector_type(2)));
typedef float f32x16 __attribute__((ext_vector_type(16)));
typedef unsigned u32x4 __attribute__((ext_vector_type(4)));
typedef unsigned u32x2 __attribute__((ext_vector_type(2)));
typedef unsigned long long u64;

constexpr int SQ = 64, DM = 576, HKV = 16, SK = 2048;
constexpr unsigned TILEB = 16384;
constexpr unsigned KR = 0, VR = 5 * TILEB;
constexpr unsigned SCR = 8 * TILEB;
constexpr unsigned LDS_BYTES = 10 * TILEB;
constexpr unsigned CODE_BYTES = 21120;
constexpr float QSCALE = 0.08838834764831845f * 1.4426950408889634f;

#define AI __attribute__((always_inline))
#define SBAR() __builtin_amdgcn_sched_barrier(0)
#define MFMA(a, b, c) __builtin_amdgcn_mfma_f32_32x32x16_bf16((a), (b), (c), 0, 0, 0)
#define VMCNT(n) asm volatile("s_waitcnt vmcnt(%0)" :: "i"(n) : "memory")
#define BARRIER() asm volatile("s_waitcnt lgkmcnt(0)\n\ts_barrier" ::: "memory")
#define BAR0() asm volatile("s_barrier" ::: "memory")
#define RLX_AGENT __ATOMIC_RELAXED, __HIP_MEMORY_SCOPE_AGENT

DI unsigned cvtpk(float lo, float hi) { f32x2 v = {lo, hi}; bf16x2 b = __builtin_convertvector(v, bf16x2); return __builtin_bit_cast(unsigned, b); }
DI float bflo(unsigned u) { return __uint_as_float(u << 16); }
DI float bfhi(unsigned u) { return __uint_as_float(u & 0xffff0000u); }
DI bf16x8 lds_b128(unsigned a) { return *(const LAS bf16x8*)(uintptr_t)a; }
DI s16x4 lds_tr(unsigned a) { return __builtin_amdgcn_ds_read_tr16_b64_v4i16((LAS s16x4*)(uintptr_t)a); }
DI bf16x8 cat(s16x4 lo, s16x4 hi) { s16x8 r = __builtin_shufflevector(lo, hi, 0, 1, 2, 3, 4, 5, 6, 7); return __builtin_bit_cast(bf16x8, r); }
DI void lds_w128(unsigned a, u32x4 v) { *(LAS u32x4*)(uintptr_t)a = v; }
template <int NT, int OFF> DI void dma1(const void* sbase, unsigned voff, unsigned m) {
    if constexpr (NT) asm volatile("s_mov_b32 m0, %2\n\ts_nop 0\n\tglobal_load_lds_dwordx4 %0, %1 offset:%3 nt" :: "v"(voff), "s"(sbase), "s"(m), "i"(OFF) : "memory");
    else asm volatile("s_mov_b32 m0, %2\n\ts_nop 0\n\tglobal_load_lds_dwordx4 %0, %1 offset:%3" :: "v"(voff), "s"(sbase), "s"(m), "i"(OFF) : "memory");
}
template <int NT, int OFF> DI void dma2(const void* sbase, unsigned v0, unsigned v1, unsigned m) {
    if constexpr (NT) asm volatile("s_mov_b32 m0, %3\n\ts_nop 0\n\tglobal_load_lds_dwordx4 %0, %2 offset:%4 nt\n\tglobal_load_lds_dwordx4 %1, %2 offset:%5 nt" :: "v"(v0), "v"(v1), "s"(sbase), "s"(m), "i"(OFF), "i"(OFF + 1024) : "memory");
    else asm volatile("s_mov_b32 m0, %3\n\ts_nop 0\n\tglobal_load_lds_dwordx4 %0, %2 offset:%4\n\tglobal_load_lds_dwordx4 %1, %2 offset:%5" :: "v"(v0), "v"(v1), "s"(sbase), "s"(m), "i"(OFF), "i"(OFF + 1024) : "memory");
}
DI f32x16 mfma_c(bf16x8 a, bf16x8 b, const f32x16& c) {
    f32x16 d; asm("s_nop 1\n\tv_mfma_f32_32x32x16_bf16 %0, %1, %2, %3" : "=&v"(d) : "v"(a), "v"(b), "v"(c)); return d; }
DI float fsub_s(float a, float b) { float r; asm("v_sub_f32_e32 %0, %1, %2" : "=v"(r) : "v"(a), "v"(b)); return r; }
DI float swapmax(float x) { auto r = __builtin_amdgcn_permlane32_swap(__float_as_uint(x), __float_as_uint(x), false, false); return __builtin_fmaxf(__uint_as_float(r[0]), __uint_as_float(r[1])); }
DI float swapadd(float x) { auto r = __builtin_amdgcn_permlane32_swap(__float_as_uint(x), __float_as_uint(x), false, false); return __uint_as_float(r[0]) + __uint_as_float(r[1]); }
#define MX3(a, b, c) __builtin_fmaxf(__builtin_fmaxf((a), (b)), (c))
DI void st_sc1(__amdgpu_buffer_rsrc_t r, unsigned off, u32x4 v) { __builtin_amdgcn_raw_buffer_store_b128(v, r, off, 0, 16); }
DI u32x4 ld_sc1(__amdgpu_buffer_rsrc_t r, unsigned off) { return __builtin_amdgcn_raw_buffer_load_b128(r, off, 0, 16); }
DI void wait_flag(GAS u64* f, u64 tag) {
    u64 v0 = __hip_atomic_load(f, RLX_AGENT);
    if (!__all(v0 == tag)) {
        v0 = __hip_atomic_load(f, RLX_AGENT);
        __builtin_amdgcn_s_sleep(8);
#pragma unroll 1
        for (unsigned spin = 0; spin < (1u << 20); ++spin) {
            const u64 v1 = __hip_atomic_load(f, RLX_AGENT);
            if (__all(v0 == tag)) break;
            v0 = __hip_atomic_load(f, RLX_AGENT);
            if (__all(v1 == tag)) break;
        }
    }
    asm volatile("" ::: "memory");
}

DI unsigned pace(unsigned tn, unsigned per) {
    unsigned nx;
    asm volatile("1:\n\ts_memrealtime s[98:99]\n\ts_waitcnt lgkmcnt(0)\n\ts_sub_i32 s99, s98, %1\n\ts_cmp_lt_i32 s99, 0\n\ts_cbranch_scc1 1b\n\ts_add_i32 %0, s98, %2"
                 : "=s"(nx) : "s"(tn), "s"(per) : "memory", "scc", "s98", "s99");
    return nx;
}

constexpr size_t WS_EPOCH = 0;
constexpr size_t WS_F1 = 65536;
constexpr size_t WS_F2 = 262144;
constexpr size_t WS_P1 = 1u << 20;
constexpr size_t WS_P2 = 12u << 20;
constexpr unsigned P2STRIDE = 16 * 36864;

using T_ = std::integral_constant<bool, true>; using F_ = std::integral_constant<bool, false>;

__global__ __launch_bounds__(512, 2) void gqa_fused(const char* X, const char* Kc, const char* Vc, const char* Wq, const char* Wo, char* Out, char* ws) {
    extern __shared__ __attribute__((aligned(16))) char smem[];
    asm volatile("" : "+s"(X), "+s"(Kc), "+s"(Vc), "+s"(Wq), "+s"(Wo), "+s"(Out), "+s"(ws));
    u64 pc0; asm volatile("s_getpc_b64 %0" : "=s"(pc0));
    const unsigned tid = threadIdx.x, lane = tid & 63;
    const unsigned wid = __builtin_amdgcn_readfirstlane(tid >> 6);
    const unsigned r32 = lane & 31, hi = lane >> 5, w4 = wid & 3, g = wid >> 2;
    const unsigned L0 = (unsigned)(uintptr_t)smem;
    const unsigned wg = blockIdx.x, xcd = wg & 7, slot = wg >> 3;
    const unsigned kvh = (xcd & 3) * 4 + (slot & 3), sp = xcd >> 2, b = slot >> 2;
    const unsigned pair = b * HKV + kvh, me = pair * 2 + sp;
    constexpr unsigned TS = 16384;
    const unsigned woff = sp * 262144 + w4 * 4096;
    const char* Kb = Kc + (size_t)pair * SK * 256 + woff;
    const char* Vb = Vc + (size_t)pair * SK * 256 + woff;
    const unsigned kv0 = (lane >> 4) * 256 + (((lane & 15) ^ (lane >> 4)) << 4), kv1 = kv0 ^ 64, kv2 = kv0 ^ 128, kv3 = kv0 ^ 192;
    const unsigned vvoff = (lane >> 4) * 256 + (((lane & 15) ^ ((lane >> 4) << 2)) << 4);
    const unsigned Lw = L0 + w4 * 1024, Lq = L0 + w4 * 4096;
    const unsigned rot = b * 2;
    auto dmaK = [&](unsigned t, unsigned sl) { t = (t + rot) & 15; dma2<1, 0>(Kb + t * TS, kv0, kv1, Lq + KR + sl * TILEB); dma2<1, 2048>(Kb + t * TS, kv2, kv3, Lq + KR + sl * TILEB); };
    auto dmaV = [&](unsigned t, unsigned sl) { t = (t + rot) & 15; dma2<1, 0>(Vb + t * TS, vvoff, vvoff, Lq + VR + sl * TILEB); dma2<1, 2048>(Vb + t * TS, vvoff, vvoff, Lq + VR + sl * TILEB); };
    const unsigned g1 = (lane >> 4) & 1, q4 = (lane & 15) >> 2, p4 = lane & 3;

    bf16x8 qf[8];
    auto touch = [&](const void* sbase, unsigned voff, unsigned dst) {
        asm volatile("s_mov_b32 m0, %2\n\ts_nop 0\n\tglobal_load_lds_dword %0, %1" :: "v"(voff), "s"(sbase), "s"(dst) : "memory");
    };
    {
        constexpr unsigned XR = 3 * TILEB, WR = 5 * TILEB;
        if (g) {
            const char* Xb = X + (size_t)b * SQ * DM * 2;
            const unsigned xvoff = (8 * w4 + (lane >> 3)) * (DM * 2) + (((lane & 7) ^ (4 * (w4 & 1) + (lane >> 4))) << 4);
            const char* Wb = Wq + (2 * kvh + (w4 >> 1)) * 256 + (size_t)(w4 & 1) * 16 * 8192;
            const unsigned wvoff = (lane >> 4) * 8192 + (((lane & 15) ^ ((lane >> 4) << 2)) << 4);
            unsigned cb = b + (kvh & 3) * 2; cb = cb >= 9 ? cb - 9 : cb;
            unsigned xb = cb;
            auto xhalf = [&](unsigned j, unsigned h) { dma1<0, 0>(Xb + xb * 128 + h * (32 * DM * 2), xvoff, Lw + XR + (j & 3) * 8192 + h * 4096); if (h) xb = xb == 8 ? 0 : xb + 1; };
            unsigned ws5 = 0;
            auto phase = [&](unsigned p) {
                xhalf((p + 1) >> 1, (p & 1) ^ 1);
                const char* src = Wb + (size_t)(cb * 2 + (p & 1)) * (32 * 8192);
                dma2<0, 0>(src, wvoff, wvoff + 31744, Lq + WR + ws5); dma2<0, 2048>(src, wvoff + 2 * 31744, wvoff + 3 * 31744, Lq + WR + ws5);
                ws5 = ws5 == 4 * TILEB ? 0 : ws5 + TILEB;
                if (p & 1) cb = cb == 8 ? 0 : cb + 1;
            };
            for (unsigned i = 0; i < (b & 3) * 4; ++i) __builtin_amdgcn_s_sleep(4);
            xhalf(0, 0);
            phase(0); phase(1); phase(2);
            VMCNT(10); BAR0();
            phase(3); phase(4);
#pragma unroll 1
            for (unsigned p = 5; p < 18; ++p) { VMCNT(15); BAR0(); phase(p); }
            VMCNT(15); BAR0(); dmaK(0, 0);
            VMCNT(14); BAR0(); dmaK(1, 1);
            VMCNT(13); BAR0(); dmaK(2, 2);
            VMCNT(12); BAR0();
            BAR0();
            dmaV(0, 0);
            VMCNT(12); BAR0();
#pragma unroll
            for (int s = 0; s < 8; ++s) qf[s] = lds_b128(L0 + SCR + (w4 * 8 + s) * 1024 + lane * 16);
            SBAR();
            dmaK(3, 3); dmaV(1, 1);
            VMCNT(16);
        } else {
            if (w4 == 0) { const unsigned ln = slot * 8 + (lane & 7); touch((const void*)(pc0 & ~127ull), ln * 128 < CODE_BYTES ? ln * 128 : 0u, Lw + KR); }
            const unsigned ps = ((p4 & 1) << 1) | (p4 >> 1);
            const unsigned wa0 = (L0 + WR + (w4 >> 1) * 8192 + (8 * hi + q4) * 256 + (q4 << 6) + g1 * 32 + ps * 8) ^ ((w4 & 1) << 7);
            const unsigned xa0 = L0 + XR + r32 * 128 + ((hi ^ ((r32 >> 1) & 7)) << 4);
            f32x16 acc[4] = {};
            bf16x8 fa[2][4], fb[2][4];
            unsigned rs5 = 0;
            auto rdA = [&](int set, unsigned p, int i) { const unsigned a = ((wa0 + rs5) ^ ((i & 1) << 6)) + (i >> 1) * 4096; fa[set][i] = cat(lds_tr(a), lds_tr(a + 1024)); };
            auto rdB = [&](int set, unsigned p, int i) { fb[set][i] = lds_b128(((xa0 + ((p >> 1) & 3) * 8192) ^ ((2 * set + (i >> 1)) << 5)) + (i & 1) * 4096); };
            auto step = [&](auto set_, auto last_, unsigned pn) AI {
                constexpr int set = decltype(set_)::value; constexpr bool last = decltype(last_)::value;
                rs5 = rs5 == 4 * TILEB ? 0 : rs5 + TILEB;
                SBAR(); BARRIER(); SBAR();
                __builtin_amdgcn_s_sleep(2);
#pragma unroll
                for (int i = 0; i < 8; ++i) {
                    acc[i & 3] = MFMA(fa[set][2 * (i >> 2) + ((i >> 1) & 1)], fb[set][2 * (i >> 2) + (i & 1)], acc[i & 3]);
                    if (!last && i < 4) { rdA(set ^ 1, pn, i); rdB(set ^ 1, pn, i); }
                    SBAR();
                }
            };
            using I0 = std::integral_constant<int, 0>; using I1 = std::integral_constant<int, 1>;
            BAR0();
#pragma unroll
            for (int i = 0; i < 4; ++i) { rdA(0, 0, i); rdB(0, 0, i); }
#pragma unroll 1
            for (unsigned p = 0; p < 16; p += 2) { if (p == 10) VMCNT(0); step(I0{}, F_{}, p + 1); step(I1{}, F_{}, p + 2); }
            step(I0{}, F_{}, 17); step(I1{}, T_{}, 0);
#pragma unroll
            for (int s = 0; s < 8; ++s) {
                u32x4 f;
#pragma unroll
                for (int e = 0; e < 4; ++e) f[e] = cvtpk(acc[s >> 1][8 * (s & 1) + 2 * e] * QSCALE, acc[s >> 1][8 * (s & 1) + 2 * e + 1] * QSCALE);
                lds_w128(L0 + SCR + (((w4 & 2) + ((s >> 1) & 1)) * 8 + (w4 & 1) * 4 + (s >> 2) * 2 + (s & 1)) * 1024 + lane * 16, f);
            }
            BARRIER();
#pragma unroll
            for (int s = 0; s < 8; ++s) qf[s] = lds_b128(L0 + SCR + (w4 * 8 + s) * 1024 + lane * 16);
        }
    }

    const unsigned kr0 = L0 + KR + r32 * 256 + ((hi ^ (r32 & 15)) << 4);
    const unsigned vr0 = L0 + VR + (4 * hi + q4) * 256 + (q4 << 6) + g1 * 32 + p4 * 8;
    const char* Wob = Wo + ((size_t)(kvh * 256 + w4 * 64) * DM + sp * 288) * 2;
    const unsigned ov0 = (lane >> 2) * (DM * 2) + (lane & 3) * 16;
    constexpr unsigned OVS = 16 * DM * 2 - 1024;
    auto pcol = [&](unsigned nb) { return sp ? (nb == 8 ? 0u : nb + 1) : nb; };
    auto dmaO1 = [&](const char* src, unsigned m, int j) {
        if (j == 0) dma1<0, 0>(src, ov0, m); else if (j == 1) dma1<0, 1024>(src, ov0 + OVS, m); else if (j == 2) dma1<0, 2048>(src, ov0 + 2 * OVS, m); else dma1<0, 3072>(src, ov0 + 3 * OVS, m);
    };
    f32x16 o[4] = {};
    f32x16 s0, s1, negm = {};
    u32x4 pf[4];
    float m = 0.f, l = 0.f;

    constexpr int LA = 6;
    auto rdK = [&](unsigned ka, int i) { return lds_b128((ka ^ ((i >> 1) << 5)) + (i & 1) * 8192); };
    auto rdV = [&](unsigned va, int i) { const unsigned a = (va ^ ((i & 3) << 6)) + (i >> 2) * 4096; return cat(lds_tr(a), lds_tr(a + 2048)); };
    auto MPH = [&](auto doqk_, auto dopv_, unsigned ksl_, unsigned vsl_) AI {
        constexpr bool DQ = decltype(doqk_)::value, DP = decltype(dopv_)::value;
        constexpr int N = 16 * (int)DQ + 16 * (int)DP, OFFV = DQ ? 16 : 0;
        const unsigned ka = kr0 + ksl_ * TILEB, va = vr0 + vsl_ * TILEB;
        bf16x8 f[N];
        auto rd = [&](int i) { if (DQ && i < 16) f[i] = rdK(ka, i); else f[i] = rdV(va, i - OFFV); };
        __builtin_amdgcn_s_setprio(1);
#pragma unroll
        for (int i = 0; i < LA; ++i) rd(i);
        SBAR();
#pragma unroll
        for (int i = 0; i < N; ++i) {
            if (i + LA < N) rd(i + LA);
            if (DQ && i < 16) {
                if (i == 0) s0 = mfma_c(f[i], qf[0], negm); else if (i == 1) s1 = mfma_c(f[i], qf[0], negm);
                else if (i & 1) s1 = MFMA(f[i], qf[i >> 1], s1); else s0 = MFMA(f[i], qf[i >> 1], s0);
            } else { const int j = i - OFFV; o[j & 3] = MFMA(f[i], __builtin_bit_cast(bf16x8, pf[j >> 2]), o[j & 3]); }
            SBAR();
        }
        __builtin_amdgcn_s_setprio(0);
    };
    auto RESCALE = [&](auto first_) AI {
        constexpr bool first = decltype(first_)::value;
        float a = MX3(s0[0], s0[1], s1[0]), c = MX3(s0[2], s0[3], s1[1]); a = MX3(a, s1[2], s1[3]);
#pragma unroll
        for (int r = 4; r < 16; r += 4) { a = MX3(a, s0[r], s0[r + 1]); c = MX3(c, s0[r + 2], s0[r + 3]); a = MX3(a, s1[r], s1[r + 1]); c = MX3(c, s1[r + 2], s1[r + 3]); }
        const float rm = swapmax(__builtin_fmaxf(a, c));
        const float dl = first ? rm : __builtin_fmaxf(rm, 0.f);
        m += dl;
#pragma unroll
        for (int r = 0; r < 16; ++r) { s0[r] = fsub_s(s0[r], dl); s1[r] = fsub_s(s1[r], dl); negm[r] = fsub_s(negm[r], dl); }
        if (!first) {
            const float f = __builtin_amdgcn_exp2f(-dl);
            l *= f;
#pragma unroll
            for (int db = 0; db < 4; ++db) o[db] *= f;
        }
    };
    auto EXPS = [&](auto&& hook) AI {
        float sa = 0.f, sb = 0.f;
#pragma unroll
        for (int k = 0; k < 8; ++k) {
            const f32x16& s = k < 4 ? s0 : s1; const int r0 = (k & 3) * 4;
            const float e0 = __builtin_amdgcn_exp2f(s[r0]), e1 = __builtin_amdgcn_exp2f(s[r0 + 1]), e2 = __builtin_amdgcn_exp2f(s[r0 + 2]), e3 = __builtin_amdgcn_exp2f(s[r0 + 3]);
            if (k == 0) { sa = e0 + e2; sb = e1 + e3; } else { sa += e0; sb += e1; sa += e2; sb += e3; }
            unsigned w0 = cvtpk(e0, e1), w1 = cvtpk(e2, e3);
            asm volatile("" : "+v"(w0), "+v"(w1), "+v"(sa), "+v"(sb));
            pf[k >> 1][(k & 1) * 2] = w0; pf[k >> 1][(k & 1) * 2 + 1] = w1;
            SBAR(); hook(k); SBAR();
        }
        return sa + sb;
    };
    auto SM = [&](auto first_, auto&& hook) AI {
        constexpr bool first = decltype(first_)::value;
        if (first) RESCALE(first_);
        float ts = EXPS(hook);
        if (!first && __builtin_expect(__any(!(ts <= 256.f)), 0)) { RESCALE(F_{}); ts = EXPS([](int) {}); }
        l += ts;
        asm volatile("" : "+v"(l));
    };
    auto inc2 = [](unsigned s) { return s >= 3 ? s - 3 : s + 2; };
    unsigned t = g;
    unsigned kq = g, vq = g;
    const char* ksrc; const char* vsrc;
    unsigned mK, mV;
    auto hookKV = [&](int j) {
        if (j == 0) dma2<1, 0>(ksrc, kv0, kv1, mK); else if (j == 2) dma2<1, 2048>(ksrc, kv2, kv3, mK);
        else if (j == 4) dma2<1, 0>(vsrc, vvoff, vvoff, mV); else if (j == 6) dma2<1, 2048>(vsrc, vvoff, vvoff, mV);
    };
    auto prep = [&]() { ksrc = Kb + ((t + 4 + rot) & 15) * TS; vsrc = Vb + ((t + 2 + rot) & 15) * TS; mK = Lq + KR + (kq ? kq - 1 : 4) * TILEB; mV = Lq + VR + inc2(vq) * TILEB; asm volatile("" : "+s"(mK), "+s"(mV), "+s"(ksrc), "+s"(vsrc)); };
    unsigned tnext = (unsigned)__builtin_amdgcn_s_memrealtime();
    asm volatile("" : "+s"(tnext));
    if (g) { SBAR(); BARRIER(); SBAR(); }
    prep();
    MPH(T_{}, F_{}, kq, 0);
    SBAR();
    if (g) VMCNT(8);
    BARRIER(); SBAR();
    SM(T_{}, hookKV); SBAR();
    VMCNT(8);
#pragma unroll 1
    while (t <= 9) {
        const unsigned vcur = vq;
        kq = inc2(kq); vq = inc2(vq); t += 2; prep();
        SBAR(); BARRIER(); SBAR();
        MPH(T_{}, T_{}, kq, vcur);
        SBAR(); BARRIER(); SBAR();
        tnext = pace(tnext, 240); SBAR();
        SM(F_{}, hookKV); SBAR();
        VMCNT(8);
    }
#pragma unroll 1
    while (t <= 13) {
        const unsigned vcur = vq;
        kq = inc2(kq); vq = inc2(vq); t += 2; prep();
        const char* oA = Wob + pcol(t == 12 ? 8 : 2 * t - 26) * 64; const char* oB = Wob + pcol(2 * t - 25) * 64;
        unsigned mA = Lq + KR + (t == 12 ? 1 : kq) * TILEB, mB = Lq + VR + (t - 12) * TILEB;
        asm volatile("" : "+s"(oA), "+s"(oB), "+s"(mA), "+s"(mB));
        SBAR(); BARRIER(); SBAR();
        MPH(T_{}, T_{}, kq, vcur);
        SBAR(); BARRIER(); SBAR();
        if (t <= 13) { hookKV(4); hookKV(6); }
        SBAR();
        SM(F_{}, [&](int j) { if (!(j & 1)) { dmaO1(oA, mA, j >> 1); if (t >= 13) dmaO1(oB, mB, j >> 1); } }); SBAR();
        if (t == 13 || t == 14) VMCNT(12); else VMCNT(8);
    }
    SBAR(); BARRIER(); SBAR();
    MPH(F_{}, T_{}, 0, vq);
    l = swapadd(l);

    const unsigned wor = (4 * hi + q4) * 64 + g1 * 32 + p4 * 8;
    bf16x8 wf[16];
    auto xslot = [](unsigned w) { return (w & 1) ? VR + (1 + (w >> 1)) * TILEB : KR + (3 + (w >> 1)) * TILEB; };
    u64 tag;
    auto TAIL = [&](auto G_) AI {
        constexpr int G = decltype(G_)::value;
        const __amdgpu_buffer_rsrc_t p1o = __builtin_amdgcn_make_buffer_rsrc(ws + WS_P1 + (size_t)(me * 8 + wid) * 4608, 0, 4608, 0x00020000);
        const __amdgpu_buffer_rsrc_t p1i = __builtin_amdgcn_make_buffer_rsrc(ws + WS_P1 + (size_t)((me ^ 1) * 8 + wid) * 4608, 0, 4608, 0x00020000);
        const unsigned xa = L0 + xslot(w4) + lane * 16, xb = L0 + ((w4 & 2) ? VR + 4 * TILEB : KR + 2 * TILEB) + (w4 & 1) * 8192 + lane * 16;
        const float inv = __builtin_amdgcn_rcpf(l);
        float lse = m + __builtin_amdgcn_logf(l);
        u32x4 f[4];
#pragma unroll
        for (int j = 0; j < 4; ++j) {
#pragma unroll
            for (int e = 0; e < 4; ++e) { const int r = 8 * (j & 1) + 2 * e; const f32x16& oo = o[2 * (G ^ 1) + (j >> 1)]; f[j][e] = cvtpk(oo[r] * inv, oo[r + 1] * inv); }
            asm volatile("" : "+v"(f[j]));
        }
        if (!G) {
            asm volatile("" : "+v"(lse));
            SBAR(); VMCNT(0);
            BARRIER(); SBAR();
#pragma unroll
            for (int ks = 0; ks < 16; ++ks) wf[ks] = cat(lds_tr(L0 + xslot(w4) + wor + ks * 1024), lds_tr(L0 + xslot(w4) + wor + ks * 1024 + 512));
            asm volatile("s_waitcnt lgkmcnt(0)" ::: "memory");
        }
        const unsigned xo = G ? xb : xa, xi = G ? xa : xb;
#pragma unroll
        for (int j = 0; j < 4; ++j) lds_w128(xo + j * 1024, f[j]);
        *(LAS float*)(uintptr_t)(xo - lane * 12 + 4096) = lse;
        if (!G) {
#pragma unroll
            for (int j = 0; j < 4; ++j) { dmaO1(Wob + pcol(6) * 64, L0 + xslot(w4) + 8192, j); dmaO1(Wob + pcol(7) * 64, L0 + xslot(w4) + 12288, j); }
        }
        BARRIER();
        const u64 epoch = __hip_atomic_load((GAS u64*)(ws + WS_EPOCH + b * 256), RLX_AGENT);
        const float lseO = *(const LAS float*)(uintptr_t)(xi - lane * 12 + 4096);
#pragma unroll
        for (int j = 0; j < 4; ++j) f[j] = *(const LAS u32x4*)(uintptr_t)(xi + j * 1024);
        const float mx = __builtin_fmaxf(lse, lseO), ea = __builtin_amdgcn_exp2f(lse - mx), eb = __builtin_amdgcn_exp2f(lseO - mx);
        const float rden = __builtin_amdgcn_rcpf(ea + eb), ca = ea * rden * inv, cb = eb * rden, lseL = mx + __builtin_amdgcn_logf(ea + eb);
        __builtin_amdgcn_raw_buffer_store_b32(__float_as_uint(lseL), p1o, 4096 + lane * 4, 0, 16);
#pragma unroll
        for (int j = 0; j < 4; ++j) {
            u32x4 w;
#pragma unroll
            for (int e = 0; e < 4; ++e) {
                const int r = 8 * (j & 1) + 2 * e; f32x16& oo = o[2 * G + (j >> 1)];
                oo[r] = oo[r] * ca + bflo(f[j][e]) * cb; oo[r + 1] = oo[r + 1] * ca + bfhi(f[j][e]) * cb;
                w[e] = cvtpk(oo[r], oo[r + 1]);
            }
            st_sc1(p1o, j * 1024 + lane * 16, w);
        }
        SBAR(); VMCNT(0); SBAR();
        tag = ((u64)__builtin_amdgcn_readfirstlane((unsigned)(epoch >> 32)) << 32 | (unsigned)__builtin_amdgcn_readfirstlane((unsigned)epoch)) * 0x9E3779B97F4A7C15ull + 0x632BE59BD9B4E019ull;
        if (lane == 0) __hip_atomic_store((GAS u64*)(ws + WS_F1 + (me * 8 + wid) * 64), tag, RLX_AGENT);
        BARRIER();
        if (G) {
#pragma unroll
            for (int ks = 0; ks < 16; ++ks) {
                const unsigned a = L0 + (w4 < 2 ? (w4 ? VR + 3 * TILEB : KR) + ks * 1024 : xslot(ks >> 2) + w4 * 4096 + (ks & 3) * 1024) + wor;
                wf[ks] = cat(lds_tr(a), lds_tr(a + 512));
            }
        }
        wait_flag((GAS u64*)(ws + WS_F1 + ((me ^ 1) * 8 + wid) * 64), tag);
        const float lseR = __uint_as_float(__builtin_amdgcn_raw_buffer_load_b32(p1i, 4096 + lane * 4, 0, 16));
#pragma unroll
        for (int j = 0; j < 4; ++j) f[j] = ld_sc1(p1i, j * 1024 + lane * 16);
        const float mx2 = __builtin_fmaxf(lseL, lseR), e0 = __builtin_amdgcn_exp2f(lseL - mx2), e1 = __builtin_amdgcn_exp2f(lseR - mx2);
        const float rd2 = __builtin_amdgcn_rcpf(e0 + e1), c0 = e0 * rd2, c1 = e1 * rd2;
#pragma unroll
        for (int j = 0; j < 4; ++j) {
            u32x4 w;
#pragma unroll
            for (int e = 0; e < 4; ++e) {
                const int r = 8 * (j & 1) + 2 * e; const f32x16& oo = o[2 * G + (j >> 1)];
                w[e] = cvtpk(oo[r] * c0 + bflo(f[j][e]) * c1, oo[r + 1] * c0 + bfhi(f[j][e]) * c1);
            }
            lds_w128(xa + (4 * G + j) * 1024, w);
        }
        BARRIER();
    };
    if (g) TAIL(std::integral_constant<int, 1>{}); else TAIL(std::integral_constant<int, 0>{});
    {
        const __amdgpu_buffer_rsrc_t p2 = __builtin_amdgcn_make_buffer_rsrc(ws + WS_P2 + (size_t)(b * 2 + sp) * P2STRIDE, 0, 16 * 36864, 0x00020000);
        auto put = [&](const f32x16& c, unsigned piece) {
#pragma unroll
            for (int u = 0; u < 2; ++u) {
                u32x4 f;
#pragma unroll
                for (int e = 0; e < 4; ++e) f[e] = cvtpk(c[8 * u + 2 * e], c[8 * u + 2 * e + 1]);
                st_sc1(p2, kvh * 36864 + (piece + u) * 1024 + lane * 16, f);
            }
        };
        constexpr int LB = 6;
        const unsigned rbf = wid & 1;
        const unsigned ob[4] = {L0 + xslot(rbf) + lane * 16, L0 + xslot(2 + rbf) + lane * 16, L0 + xslot(rbf ^ 1) + lane * 16, L0 + xslot(2 + (rbf ^ 1)) + lane * 16};
        {
            bf16x8 f[32]; f32x16 c = {}, c2 = {};
            auto rd = [&](int i) { f[i] = lds_b128(ob[i >> 3] + (i & 7) * 1024); };
#pragma unroll
            for (int i = 0; i < LB; ++i) rd(i);
            SBAR();
#pragma unroll
            for (int i = 0; i < 32; ++i) {
                if (i + LB < 32) rd(i + LB);
                if (i < 16) c = MFMA(wf[i], f[i], c); else c2 = MFMA(wf[i - 16], f[i], c2);
                if (i == 19) put(c, wid * 4 + rbf * 2);
                SBAR();
            }
            if (wid < 2) {
                bf16x8 fa[16]; c = f32x16{};
                auto rd8 = [&](int i) { const unsigned a = L0 + KR + 1 * TILEB + wor + i * 1024; fa[i] = cat(lds_tr(a), lds_tr(a + 512)); f[i] = lds_b128(ob[i >> 3] + (i & 7) * 1024); };
#pragma unroll
                for (int i = 0; i < 4; ++i) rd8(i);
                SBAR();
#pragma unroll
                for (int i = 0; i < 16; ++i) {
                    if (i + 4 < 16) rd8(i + 4);
                    c = MFMA(fa[i], f[i], c);
                    if (i == 3) put(c2, wid * 4 + (rbf ^ 1) * 2);
                    SBAR();
                }
                put(c, 32 + wid * 2);
            } else put(c2, wid * 4 + (rbf ^ 1) * 2);
        }
        VMCNT(0);
        GAS u64* f2 = (GAS u64*)(ws + WS_F2);
        {
            const unsigned cons = lane < 2 ? wid * 2 + lane : wid * 8 + (lane - 2);
            if (lane < (wid < 2 ? 10u : 2u)) __hip_atomic_store(f2 + (size_t)((b * HKV + cons) * 2 + sp) * 32 + (lane < 2 ? 0 : 16) + kvh, tag, RLX_AGENT);
        }
        {
            if (wid == 7) wait_flag(f2 + (size_t)me * 32 + (lane & 31), tag);
            const unsigned sg = lane >> 4, c16 = lane & 15;
            const unsigned cc = kvh * 128 + wid * 16 + c16, c8 = 2048 + kvh * 16 + c16;
            auto oaddr = [&](unsigned c) {
                const unsigned piece = c >> 6, ln = c & 63, nb = piece >> 2, rb = (piece >> 1) & 1, u = piece & 1;
                return Out + ((size_t)(b * 64 + rb * 32 + (ln & 31)) * DM + sp * 288 + pcol(nb) * 32 + u * 16 + (ln >> 5) * 4 + (sg >> 1) * 8 + (sg & 1) * 2) * 2;
            };
            char* op = oaddr(cc); char* op8 = oaddr(c8);
            asm volatile("" : "+v"(op), "+v"(op8));
            BAR0();
            auto fin = [&](const u32x4* v, char* o_) {
                float a[8];
#pragma unroll
                for (int e = 0; e < 4; ++e) { a[2 * e] = (bflo(v[0][e]) + bflo(v[1][e])) + (bflo(v[2][e]) + bflo(v[3][e])); a[2 * e + 1] = (bfhi(v[0][e]) + bfhi(v[1][e])) + (bfhi(v[2][e]) + bfhi(v[3][e])); }
                float h[4], q[2];
#pragma unroll
                for (int e = 0; e < 4; ++e) { auto r = __builtin_amdgcn_permlane32_swap(__float_as_uint(a[e]), __float_as_uint(a[e + 4]), false, false); h[e] = __uint_as_float(r[0]) + __uint_as_float(r[1]); }
#pragma unroll
                for (int e = 0; e < 2; ++e) { auto r = __builtin_amdgcn_permlane16_swap(__float_as_uint(h[e]), __float_as_uint(h[e + 2]), false, false); q[e] = __uint_as_float(r[0]) + __uint_as_float(r[1]); }
                *(GAS unsigned*)o_ = cvtpk(q[0], q[1]);
            };
            u32x4 v[4], v8[4];
#pragma unroll
            for (int s = 0; s < 4; ++s) v[s] = ld_sc1(p2, (sg * 4 + s) * 36864 + cc * 16);
            if (wid == 6) {
#pragma unroll
                for (int s = 0; s < 4; ++s) v8[s] = ld_sc1(p2, (sg * 4 + s) * 36864 + c8 * 16);
            }
            if (wid == 0 && kvh == 0 && sp == 0 && lane == 0) __hip_atomic_store((GAS u64*)(ws + WS_EPOCH + b * 256), tag, RLX_AGENT);
            fin(v, op);
            if (wid == 6) fin(v8, op8);
        }
    }
}

extern "C" void kernel_launch(void* const* d_in, const int* in_sizes, int n_in, void* d_out, int out_size, void* d_ws, size_t ws_size, hipStream_t stream) {
    (void)hipFuncSetAttribute((const void*)gqa_fused, hipFuncAttributeMaxDynamicSharedMemorySize, LDS_BYTES);
    hipLaunchKernelGGL(gqa_fused, dim3(256), dim3(512), LDS_BYTES, stream, (const char*)d_in[0], (const char*)d_in[1], (const char*)d_in[2], (const char*)d_in[3],
                       (const char*)d_in[4], (char*)d_out, (char*)d_ws);
}
```

```cpp
#include <hip/hip_runtime.h>
#include <stdint.h>
#include <type_traits>

#define DI __device__ __forceinline__
#define LAS __attribute__((address_space(3)))
#define GAS __attribute__((address_space(1)))
typedef __bf16 bf16x8 __attribute__((ext_vector_type(8)));
typedef __bf16 bf16x2 __attribute__((ext_vector_type(2)));
typedef short s16x4 __attribute__((ext_vector_type(4)));
typedef short s16x8 __attribute__((ext_vector_type(8)));
typedef float f32x2 __attribute__((ext_vector_type(2)));
typedef float f32x16 __attribute__((ext_vector_type(16)));
typedef unsigned u32x4 __attribute__((ext_vector_type(4)));
typedef unsigned long long u64;

constexpr int SQ = 64, DM = 576, HKV = 16, SK = 2048;
constexpr unsigned TILEB = 16384;
constexpr unsigned KR = 0, VR = 5 * TILEB;
constexpr unsigned SCR = 8 * TILEB;
constexpr unsigned LDS_BYTES = 10 * TILEB;
constexpr unsigned CODE_BYTES = 21120;
constexpr float QSCALE = 0.08838834764831845f * 1.4426950408889634f;

#define AI __attribute__((always_inline))
#define SBAR() __builtin_amdgcn_sched_barrier(0)
#define MFMA(a, b, c) __builtin_amdgcn_mfma_f32_32x32x16_bf16((a), (b), (c), 0, 0, 0)
#define VMCNT(n) asm volatile("s_waitcnt vmcnt(%0)" :: "i"(n) : "memory")
#define BARRIER() asm volatile("s_waitcnt lgkmcnt(0)\n\ts_barrier" ::: "memory")
#define BAR0() asm volatile("s_barrier" ::: "memory")
#define RLX_AGENT __ATOMIC_RELAXED, __HIP_MEMORY_SCOPE_AGENT

DI unsigned cvtpk(float lo, float hi) { f32x2 v = {lo, hi}; bf16x2 b = __builtin_convertvector(v, bf16x2); return __builtin_bit_cast(unsigned, b); }
DI float bflo(unsigned u) { return __uint_as_float(u << 16); }
DI float bfhi(unsigned u) { return __uint_as_float(u & 0xffff0000u); }
DI bf16x8 lds_b128(unsigned a) { return *(const LAS bf16x8*)(uintptr_t)a; }
DI s16x4 lds_tr(unsigned a) { return __builtin_amdgcn_ds_read_tr16_b64_v4i16((LAS s16x4*)(uintptr_t)a); }
DI bf16x8 cat(s16x4 lo, s16x4 hi) { s16x8 r = __builtin_shufflevector(lo, hi, 0, 1, 2, 3, 4, 5, 6, 7); return __builtin_bit_cast(bf16x8, r); }
DI void lds_w128(unsigned a, u32x4 v) { *(LAS u32x4*)(uintptr_t)a = v; }
template <int NT, int OFF> DI void dma1(const void* sbase, unsigned voff, unsigned m) {
    if constexpr (NT) asm volatile("s_mov_b32 m0, %2\n\ts_nop 0\n\tglobal_load_lds_dwordx4 %0, %1 offset:%3 nt" :: "v"(voff), "s"(sbase), "s"(m), "i"(OFF) : "memory");
    else asm volatile("s_mov_b32 m0, %2\n\ts_nop 0\n\tglobal_load_lds_dwordx4 %0, %1 offset:%3" :: "v"(voff), "s"(sbase), "s"(m), "i"(OFF) : "memory");
}
template <int NT, int OFF> DI void dma2(const void* sbase, unsigned v0, unsigned v1, unsigned m) {
    if constexpr (NT) asm volatile("s_mov_b32 m0, %3\n\ts_nop 0\n\tglobal_load_lds_dwordx4 %0, %2 offset:%4 nt\n\tglobal_load_lds_dwordx4 %1, %2 offset:%5 nt" :: "v"(v0), "v"(v1), "s"(sbase), "s"(m), "i"(OFF), "i"(OFF + 1024) : "memory");
    else asm volatile("s_mov_b32 m0, %3\n\ts_nop 0\n\tglobal_load_lds_dwordx4 %0, %2 offset:%4\n\tglobal_load_lds_dwordx4 %1, %2 offset:%5" :: "v"(v0), "v"(v1), "s"(sbase), "s"(m), "i"(OFF), "i"(OFF + 1024) : "memory");
}
DI f32x16 mfma_c(bf16x8 a, bf16x8 b, const f32x16& c) {
    f32x16 d; asm("s_nop 1\n\tv_mfma_f32_32x32x16_bf16 %0, %1, %2, %3" : "=&v"(d) : "v"(a), "v"(b), "v"(c)); return d; }
DI float fsub_s(float a, float b) { float r; asm("v_sub_f32_e32 %0, %1, %2" : "=v"(r) : "v"(a), "v"(b)); return r; }
DI float swapmax(float x) { auto r = __builtin_amdgcn_permlane32_swap(__float_as_uint(x), __float_as_uint(x), false, false); return __builtin_fmaxf(__uint_as_float(r[0]), __uint_as_float(r[1])); }
DI float swapadd(float x) { auto r = __builtin_amdgcn_permlane32_swap(__float_as_uint(x), __float_as_uint(x), false, false); return __uint_as_float(r[0]) + __uint_as_float(r[1]); }
#define MX3(a, b, c) __builtin_fmaxf(__builtin_fmaxf((a), (b)), (c))
DI void st_sc1(__amdgpu_buffer_rsrc_t r, unsigned off, u32x4 v) { __builtin_amdgcn_raw_buffer_store_b128(v, r, off, 0, 16); }
DI u32x4 ld_sc1(__amdgpu_buffer_rsrc_t r, unsigned off) { return __builtin_amdgcn_raw_buffer_load_b128(r, off, 0, 16); }
DI void wait_flag(GAS u64* f, u64 tag) {
    u64 v0 = __hip_atomic_load(f, RLX_AGENT);
    if (!__all(v0 == tag)) {
        v0 = __hip_atomic_load(f, RLX_AGENT);
        __builtin_amdgcn_s_sleep(8);
#pragma unroll 1
        for (unsigned spin = 0; spin < (1u << 20); ++spin) {
            const u64 v1 = __hip_atomic_load(f, RLX_AGENT);
            if (__all(v0 == tag)) break;
            v0 = __hip_atomic_load(f, RLX_AGENT);
            if (__all(v1 == tag)) break;
        }
    }
    asm volatile("" ::: "memory");
}

DI unsigned pace(unsigned tn, unsigned per) {
    unsigned nx;
    asm volatile("1:\n\ts_memrealtime s[98:99]\n\ts_waitcnt lgkmcnt(0)\n\ts_sub_i32 s99, s98, %1\n\ts_cmp_lt_i32 s99, 0\n\ts_cbranch_scc1 1b\n\ts_add_i32 %0, s98, %2"
                 : "=s"(nx) : "s"(tn), "s"(per) : "memory", "scc", "s98", "s99");
    return nx;
}

constexpr size_t WS_EPOCH = 0;
constexpr size_t WS_F1 = 65536;
constexpr size_t WS_F2 = 262144;
constexpr size_t WS_P1 = 1u << 20;
constexpr size_t WS_P2 = 12u << 20;
constexpr unsigned P2STRIDE = 16 * 36864;

using T_ = std::integral_constant<bool, true>; using F_ = std::integral_constant<bool, false>;

__global__ __launch_bounds__(512, 2) void gqa_fused(const char* X, const char* Kc, const char* Vc, const char* Wq, const char* Wo, char* Out, char* ws) {
    extern __shared__ __attribute__((aligned(16))) char smem[];
    asm volatile("" : "+s"(X), "+s"(Kc), "+s"(Vc), "+s"(Wq), "+s"(Wo), "+s"(Out), "+s"(ws));
    u64 pc0; asm volatile("s_getpc_b64 %0" : "=s"(pc0));
    const unsigned tid = threadIdx.x, lane = tid & 63;
    const unsigned wid = __builtin_amdgcn_readfirstlane(tid >> 6);
    const unsigned r32 = lane & 31, hi = lane >> 5, w4 = wid & 3, g = wid >> 2;
    const unsigned L0 = (unsigned)(uintptr_t)smem;
    const unsigned wg = blockIdx.x, xcd = wg & 7, slot = wg >> 3;
    const unsigned kvh = (xcd & 3) * 4 + (slot & 3), sp = xcd >> 2, b = slot >> 2;
    const unsigned pair = b * HKV + kvh, me = pair * 2 + sp;
    constexpr unsigned TS = 16384;
    const unsigned woff = sp * 262144 + w4 * 4096;
    const char* Kb = Kc + (size_t)pair * SK * 256 + woff;
    const char* Vb = Vc + (size_t)pair * SK * 256 + woff;
    const unsigned kv0 = (lane >> 4) * 256 + (((lane & 15) ^ (lane >> 4)) << 4), kv1 = kv0 ^ 64, kv2 = kv0 ^ 128, kv3 = kv0 ^ 192;
    const unsigned vvoff = (lane >> 4) * 256 + (((lane & 15) ^ ((lane >> 4) << 2)) << 4);
    const unsigned Lw = L0 + w4 * 1024, Lq = L0 + w4 * 4096;
    const unsigned rot = b * 2;
    auto dmaK = [&](unsigned t, unsigned sl) { t = (t + rot) & 15; dma2<1, 0>(Kb + t * TS, kv0, kv1, Lq + KR + sl * TILEB); dma2<1, 2048>(Kb + t * TS, kv2, kv3, Lq + KR + sl * TILEB); };
    auto dmaV = [&](unsigned t, unsigned sl) { t = (t + rot) & 15; dma2<1, 0>(Vb + t * TS, vvoff, vvoff, Lq + VR + sl * TILEB); dma2<1, 2048>(Vb + t * TS, vvoff, vvoff, Lq + VR + sl * TILEB); };
    const unsigned g1 = (lane >> 4) & 1, q4 = (lane & 15) >> 2, p4 = lane & 3;

    bf16x8 qf[8];
    auto touch = [&](const void* sbase, unsigned voff, unsigned dst) {
        asm volatile("s_mov_b32 m0, %2\n\ts_nop 0\n\tglobal_load_lds_dword %0, %1" :: "v"(voff), "s"(sbase), "s"(dst) : "memory");
    };
    {
        constexpr unsigned XR = 3 * TILEB, WR = 5 * TILEB;
        if (g) {
            const char* Xb = X + (size_t)b * SQ * DM * 2;
            const unsigned xvoff = (8 * w4 + (lane >> 3)) * (DM * 2) + (((lane & 7) ^ (4 * (w4 & 1) + (lane >> 4))) << 4);
            const char* Wb = Wq + (2 * kvh + (w4 >> 1)) * 256 + (size_t)(w4 & 1) * 16 * 8192;
            const unsigned wvoff = (lane >> 4) * 8192 + (((lane & 15) ^ ((lane >> 4) << 2)) << 4);
            unsigned cb = b + (kvh & 3) * 2; cb = cb >= 9 ? cb - 9 : cb;
            unsigned xb = cb;
            auto xhalf = [&](unsigned j, unsigned h) { dma1<0, 0>(Xb + xb * 128 + h * (32 * DM * 2), xvoff, Lw + XR + (j & 3) * 8192 + h * 4096); if (h) xb = xb == 8 ? 0 : xb + 1; };
            unsigned wsl = 0;
            auto phase = [&](unsigned p) {
                xhalf((p + 1) >> 1, (p & 1) ^ 1);
                const char* src = Wb + (size_t)(cb * 2 + (p & 1)) * (32 * 8192);
                dma2<0, 0>(src, wvoff, wvoff + 31744, Lq + WR + wsl); dma2<0, 2048>(src, wvoff + 2 * 31744, wvoff + 3 * 31744, Lq + WR + wsl);
                wsl = wsl == 4 * TILEB ? 0 : wsl + TILEB;
                if (p & 1) cb = cb == 8 ? 0 : cb + 1;
            };
            for (unsigned i = 0; i < (b & 3) * 4; ++i) __builtin_amdgcn_s_sleep(4);
            xhalf(0, 0);
            phase(0); phase(1); phase(2);
            VMCNT(10); BAR0();
            phase(3); phase(4);
#pragma unroll 1
            for (unsigned p = 5; p < 18; ++p) { VMCNT(15); BAR0(); phase(p); }
            VMCNT(15); BAR0(); dmaK(0, 0);
            VMCNT(14); BAR0(); dmaK(1, 1);
            VMCNT(13); BAR0(); dmaK(2, 2);
            VMCNT(12); BAR0();
            BAR0();
            dmaV(0, 0);
            VMCNT(12); BAR0();
#pragma unroll
            for (int s = 0; s < 8; ++s) qf[s] = lds_b128(L0 + SCR + (w4 * 8 + s) * 1024 + lane * 16);
            SBAR();
            dmaK(3, 3); dmaV(1, 1);
            VMCNT(16);
        } else {
            if (w4 == 0) { const unsigned ln = slot * 8 + (lane & 7); touch((const void*)(pc0 & ~127ull), ln * 128 < CODE_BYTES ? ln * 128 : 0u, Lw + KR); }
            const unsigned ps = ((p4 & 1) << 1) | (p4 >> 1);
            const unsigned wa0 = (L0 + WR + (w4 >> 1) * 8192 + (8 * hi + q4) * 256 + (q4 << 6) + g1 * 32 + ps * 8) ^ ((w4 & 1) << 7);
            const unsigned xa0 = L0 + XR + r32 * 128 + ((hi ^ ((r32 >> 1) & 7)) << 4);
            f32x16 acc[4] = {};
            bf16x8 fa[2][4], fb[2][4];
            unsigned rsl = 0;
            auto rdA = [&](int set, unsigned p, int i) { const unsigned a = ((wa0 + rsl) ^ ((i & 1) << 6)) + (i >> 1) * 4096; fa[set][i] = cat(lds_tr(a), lds_tr(a + 1024)); };
            auto rdB = [&](int set, unsigned p, int i) { fb[set][i] = lds_b128(((xa0 + ((p >> 1) & 3) * 8192) ^ ((2 * set + (i >> 1)) << 5)) + (i & 1) * 4096); };
            auto step = [&](auto set_, auto last_, unsigned pn) AI {
                constexpr int set = decltype(set_)::value; constexpr bool last = decltype(last_)::value;
                rsl = rsl == 4 * TILEB ? 0 : rsl + TILEB;
                SBAR(); BARRIER(); SBAR();
                __builtin_amdgcn_s_sleep(2);
#pragma unroll
                for (int i = 0; i < 8; ++i) {
                    acc[i & 3] = MFMA(fa[set][2 * (i >> 2) + ((i >> 1) & 1)], fb[set][2 * (i >> 2) + (i & 1)], acc[i & 3]);
                    if (!last && i < 4) { rdA(set ^ 1, pn, i); rdB(set ^ 1, pn, i); }
                    SBAR();
                }
            };
            using I0 = std::integral_constant<int, 0>; using I1 = std::integral_constant<int, 1>;
            BAR0();
#pragma unroll
            for (int i = 0; i < 4; ++i) { rdA(0, 0, i); rdB(0, 0, i); }
#pragma unroll 1
            for (unsigned p = 0; p < 16; p += 2) { if (p == 10) VMCNT(0); step(I0{}, F_{}, p + 1); step(I1{}, F_{}, p + 2); }
            step(I0{}, F_{}, 17); step(I1{}, T_{}, 0);
#pragma unroll
            for (int s = 0; s < 8; ++s) {
                u32x4 f;
#pragma unroll
                for (int e = 0; e < 4; ++e) f[e] = cvtpk(acc[s >> 1][8 * (s & 1) + 2 * e] * QSCALE, acc[s >> 1][8 * (s & 1) + 2 * e + 1] * QSCALE);
                lds_w128(L0 + SCR + (((w4 & 2) + ((s >> 1) & 1)) * 8 + (w4 & 1) * 4 + (s >> 2) * 2 + (s & 1)) * 1024 + lane * 16, f);
            }
            BARRIER();
#pragma unroll
            for (int s = 0; s < 8; ++s) qf[s] = lds_b128(L0 + SCR + (w4 * 8 + s) * 1024 + lane * 16);
        }
    }

    const unsigned kr0 = L0 + KR + r32 * 256 + ((hi ^ (r32 & 15)) << 4);
    const unsigned vr0 = L0 + VR + (4 * hi + q4) * 256 + (q4 << 6) + g1 * 32 + p4 * 8;
    const char* Wob = Wo + ((size_t)(kvh * 256 + w4 * 64) * DM + sp * 288) * 2;
    const unsigned ov0 = (lane >> 2) * (DM * 2) + (lane & 3) * 16;
    constexpr unsigned OVS = 16 * DM * 2 - 1024;
    auto pcol = [&](unsigned nb) { return sp ? (nb == 8 ? 0u : nb + 1) : nb; };
    auto dmaO1 = [&](const char* src, unsigned m, int j) {
        if (j == 0) dma1<0, 0>(src, ov0, m); else if (j == 1) dma1<0, 1024>(src, ov0 + OVS, m); else if (j == 2) dma1<0, 2048>(src, ov0 + 2 * OVS, m); else dma1<0, 3072>(src, ov0 + 3 * OVS, m);
    };
    f32x16 o[4] = {};
    f32x16 s0, s1, negm = {};
    u32x4 pf[4];
    float m = 0.f, l = 0.f;

    constexpr int LA = 6;
    auto rdK = [&](unsigned ka, int i) { return lds_b128((ka ^ ((i >> 1) << 5)) + (i & 1) * 8192); };
    auto rdV = [&](unsigned va, int i) { const unsigned a = (va ^ ((i & 3) << 6)) + (i >> 2) * 4096; return cat(lds_tr(a), lds_tr(a + 2048)); };
    auto MPH = [&](auto doqk_, auto dopv_, unsigned ksl_, unsigned vsl_) AI {
        constexpr bool DQ = decltype(doqk_)::value, DP = decltype(dopv_)::value;
        constexpr int N = 16 * (int)DQ + 16 * (int)DP, OFFV = DQ ? 16 : 0;
        const unsigned ka = kr0 + ksl_ * TILEB, va = vr0 + vsl_ * TILEB;
        bf16x8 f[N];
        auto rd = [&](int i) { if (DQ && i < 16) f[i] = rdK(ka, i); else f[i] = rdV(va, i - OFFV); };
        __builtin_amdgcn_s_setprio(1);
#pragma unroll
        for (int i = 0; i < LA; ++i) rd(i);
        SBAR();
#pragma unroll
        for (int i = 0; i < N; ++i) {
            if (i + LA < N) rd(i + LA);
            if (DQ && i < 16) {
                if (i == 0) s0 = mfma_c(f[i], qf[0], negm); else if (i == 1) s1 = mfma_c(f[i], qf[0], negm);
                else if (i & 1) s1 = MFMA(f[i], qf[i >> 1], s1); else s0 = MFMA(f[i], qf[i >> 1], s0);
            } else { const int j = i - OFFV; o[j & 3] = MFMA(f[i], __builtin_bit_cast(bf16x8, pf[j >> 2]), o[j & 3]); }
            SBAR();
        }
        __builtin_amdgcn_s_setprio(0);
    };
    auto RESCALE = [&](auto first_) AI {
        constexpr bool first = decltype(first_)::value;
        float a = MX3(s0[0], s0[1], s1[0]), c = MX3(s0[2], s0[3], s1[1]); a = MX3(a, s1[2], s1[3]);
#pragma unroll
        for (int r = 4; r < 16; r += 4) { a = MX3(a, s0[r], s0[r + 1]); c = MX3(c, s0[r + 2], s0[r + 3]); a = MX3(a, s1[r], s1[r + 1]); c = MX3(c, s1[r + 2], s1[r + 3]); }
        const float rm = swapmax(__builtin_fmaxf(a, c));
        const float dl = first ? rm : __builtin_fmaxf(rm, 0.f);
        m += dl;
#pragma unroll
        for (int r = 0; r < 16; ++r) { s0[r] = fsub_s(s0[r], dl); s1[r] = fsub_s(s1[r], dl); negm[r] = fsub_s(negm[r], dl); }
        if (!first) {
            const float f = __builtin_amdgcn_exp2f(-dl);
            l *= f;
#pragma unroll
            for (int db = 0; db < 4; ++db) o[db] *= f;
        }
    };
    auto EXPS = [&](auto&& hook) AI {
        float sa = 0.f, sb = 0.f;
#pragma unroll
        for (int k = 0; k < 8; ++k) {
            const f32x16& s = k < 4 ? s0 : s1; const int r0 = (k & 3) * 4;
            const float e0 = __builtin_amdgcn_exp2f(s[r0]), e1 = __builtin_amdgcn_exp2f(s[r0 + 1]), e2 = __builtin_amdgcn_exp2f(s[r0 + 2]), e3 = __builtin_amdgcn_exp2f(s[r0 + 3]);
            if (k == 0) { sa = e0 + e2; sb = e1 + e3; } else { sa += e0; sb += e1; sa += e2; sb += e3; }
            unsigned w0 = cvtpk(e0, e1), w1 = cvtpk(e2, e3);
            asm volatile("" : "+v"(w0), "+v"(w1), "+v"(sa), "+v"(sb));
            pf[k >> 1][(k & 1) * 2] = w0; pf[k >> 1][(k & 1) * 2 + 1] = w1;
            SBAR(); hook(k); SBAR();
        }
        return sa + sb;
    };
    auto SM = [&](auto first_, auto&& hook) AI {
        constexpr bool first = decltype(first_)::value;
        if (first) RESCALE(first_);
        float ts = EXPS(hook);
        if (!first && __builtin_expect(__any(!(ts <= 256.f)), 0)) { RESCALE(F_{}); ts = EXPS([](int) {}); }
        l += ts;
        asm volatile("" : "+v"(l));
    };
    auto inc2 = [](unsigned s) { return s >= 3 ? s - 3 : s + 2; };
    unsigned t = g;
    unsigned kq = g, vq = g;
    const char* ksrc; const char* vsrc;
    unsigned mK, mV;
    auto hookKV = [&](int j) {
        if (j == 0) dma2<1, 0>(ksrc, kv0, kv1, mK); else if (j == 2) dma2<1, 2048>(ksrc, kv2, kv3, mK);
        else if (j == 4) dma2<1, 0>(vsrc, vvoff, vvoff, mV); else if (j == 6) dma2<1, 2048>(vsrc, vvoff, vvoff, mV);
    };
    auto prep = [&]() { ksrc = Kb + ((t + 4 + rot) & 15) * TS; vsrc = Vb + ((t + 2 + rot) & 15) * TS; mK = Lq + KR + (kq ? kq - 1 : 4) * TILEB; mV = Lq + VR + inc2(vq) * TILEB; asm volatile("" : "+s"(mK), "+s"(mV), "+s"(ksrc), "+s"(vsrc)); };
    unsigned tnext = (unsigned)__builtin_amdgcn_s_memrealtime();
    asm volatile("" : "+s"(tnext));
    if (g) { SBAR(); BARRIER(); SBAR(); }
    prep();
    MPH(T_{}, F_{}, kq, 0);
    SBAR();
    if (g) VMCNT(8);
    BARRIER(); SBAR();
    SM(T_{}, hookKV); SBAR();
    VMCNT(8);
#pragma unroll 1
    while (t <= 9) {
        const unsigned vcur = vq;
        kq = inc2(kq); vq = inc2(vq); t += 2; prep();
        SBAR(); BARRIER(); SBAR();
        MPH(T_{}, T_{}, kq, vcur);
        SBAR(); BARRIER(); SBAR();
        tnext = pace(tnext, 240); SBAR();
        SM(F_{}, hookKV); SBAR();
        VMCNT(8);
    }
#pragma unroll 1
    while (t <= 13) {
        const unsigned vcur = vq;
        kq = inc2(kq); vq = inc2(vq); t += 2; prep();
        const char* oA = Wob + pcol(t == 12 ? 8 : 2 * t - 26) * 64; const char* oB = Wob + pcol(2 * t - 25) * 64;
        unsigned mA = Lq + KR + (t == 12 ? 1 : kq) * TILEB, mB = Lq + VR + (t - 12) * TILEB;
        asm volatile("" : "+s"(oA), "+s"(oB), "+s"(mA), "+s"(mB));
        SBAR(); BARRIER(); SBAR();
        MPH(T_{}, T_{}, kq, vcur);
        SBAR(); BARRIER(); SBAR();
        if (t <= 13) { hookKV(4); hookKV(6); }
        SBAR();
        SM(F_{}, [&](int j) { if (!(j & 1)) { dmaO1(oA, mA, j >> 1); if (t >= 13) dmaO1(oB, mB, j >> 1); } }); SBAR();
        if (t == 13 || t == 14) VMCNT(12); else VMCNT(8);
    }
    SBAR(); BARRIER(); SBAR();
    MPH(F_{}, T_{}, 0, vq);
    l = swapadd(l);

    const unsigned wor = (4 * hi + q4) * 64 + g1 * 32 + p4 * 8;
    bf16x8 wf[16];
    auto xslot = [](unsigned w) { return (w & 1) ? VR + (1 + (w >> 1)) * TILEB : KR + (3 + (w >> 1)) * TILEB; };
    u64 tag;
    auto TAIL = [&](auto G_) AI {
        constexpr int G = decltype(G_)::value;
        const __amdgpu_buffer_rsrc_t p1o = __builtin_amdgcn_make_buffer_rsrc(ws + WS_P1 + (size_t)(me * 8 + wid) * 4608, 0, 4608, 0x00020000);
        const __amdgpu_buffer_rsrc_t p1i = __builtin_amdgcn_make_buffer_rsrc(ws + WS_P1 + (size_t)((me ^ 1) * 8 + wid) * 4608, 0, 4608, 0x00020000);
        const unsigned xa = L0 + xslot(w4) + lane * 16, xb = L0 + ((w4 & 2) ? VR + 4 * TILEB : KR + 2 * TILEB) + (w4 & 1) * 8192 + lane * 16;
        const float inv = __builtin_amdgcn_rcpf(l);
        float lse = m + __builtin_amdgcn_logf(l);
        u32x4 f[4];
#pragma unroll
        for (int j = 0; j < 4; ++j) {
#pragma unroll
            for (int e = 0; e < 4; ++e) { const int r = 8 * (j & 1) + 2 * e; const f32x16& oo = o[2 * (G ^ 1) + (j >> 1)]; f[j][e] = cvtpk(oo[r] * inv, oo[r + 1] * inv); }
            asm volatile("" : "+v"(f[j]));
        }
        if (!G) {
            asm volatile("" : "+v"(lse));
            SBAR(); VMCNT(0);
            BARRIER(); SBAR();
#pragma unroll
            for (int ks = 0; ks < 16; ++ks) wf[ks] = cat(lds_tr(L0 + xslot(w4) + wor + ks * 1024), lds_tr(L0 + xslot(w4) + wor + ks * 1024 + 512));
            asm volatile("s_waitcnt lgkmcnt(0)" ::: "memory");
        }
        const unsigned xo = G ? xb : xa, xi = G ? xa : xb;
#pragma unroll
        for (int j = 0; j < 4; ++j) lds_w128(xo + j * 1024, f[j]);
        *(LAS float*)(uintptr_t)(xo - lane * 12 + 4096) = lse;
        if (!G) {
#pragma unroll
            for (int j = 0; j < 4; ++j) { dmaO1(Wob + pcol(6) * 64, L0 + xslot(w4) + 8192, j); dmaO1(Wob + pcol(7) * 64, L0 + xslot(w4) + 12288, j); }
        }
        BARRIER();
        const u64 epoch = __hip_atomic_load((GAS u64*)(ws + WS_EPOCH + b * 256), RLX_AGENT);
        const float lseO = *(const LAS float*)(uintptr_t)(xi - lane * 12 + 4096);
#pragma unroll
        for (int j = 0; j < 4; ++j) f[j] = *(const LAS u32x4*)(uintptr_t)(xi + j * 1024);
        const float mx = __builtin_fmaxf(lse, lseO), ea = __builtin_amdgcn_exp2f(lse - mx), eb = __builtin_amdgcn_exp2f(lseO - mx);
        const float rden = __builtin_amdgcn_rcpf(ea + eb), ca = ea * rden * inv, cb = eb * rden, lseL = mx + __builtin_amdgcn_logf(ea + eb);
        __builtin_amdgcn_raw_buffer_store_b32(__float_as_uint(lseL), p1o, 4096 + lane * 4, 0, 16);
#pragma unroll
        for (int j = 0; j < 4; ++j) {
            u32x4 w;
#pragma unroll
            for (int e = 0; e < 4; ++e) {
                const int r = 8 * (j & 1) + 2 * e; f32x16& oo = o[2 * G + (j >> 1)];
                oo[r] = oo[r] * ca + bflo(f[j][e]) * cb; oo[r + 1] = oo[r + 1] * ca + bfhi(f[j][e]) * cb;
                w[e] = cvtpk(oo[r], oo[r + 1]);
            }
            st_sc1(p1o, j * 1024 + lane * 16, w);
        }
        SBAR(); VMCNT(0); SBAR();
        tag = ((u64)__builtin_amdgcn_readfirstlane((unsigned)(epoch >> 32)) << 32 | (unsigned)__builtin_amdgcn_readfirstlane((unsigned)epoch)) * 0x9E3779B97F4A7C15ull + 0x632BE59BD9B4E019ull;
        if (lane == 0) __hip_atomic_store((GAS u64*)(ws + WS_F1 + (me * 8 + wid) * 64), tag, RLX_AGENT);
        BARRIER();
        if (G) {
#pragma unroll
            for (int ks = 0; ks < 16; ++ks) {
                const unsigned a = L0 + (w4 < 2 ? (w4 ? VR + 3 * TILEB : KR) + ks * 1024 : xslot(ks >> 2) + w4 * 4096 + (ks & 3) * 1024) + wor;
                wf[ks] = cat(lds_tr(a), lds_tr(a + 512));
            }
        }
        wait_flag((GAS u64*)(ws + WS_F1 + ((me ^ 1) * 8 + wid) * 64), tag);
        const float lseR = __uint_as_float(__builtin_amdgcn_raw_buffer_load_b32(p1i, 4096 + lane * 4, 0, 16));
#pragma unroll
        for (int j = 0; j < 4; ++j) f[j] = ld_sc1(p1i, j * 1024 + lane * 16);
        const float mx2 = __builtin_fmaxf(lseL, lseR), e0 = __builtin_amdgcn_exp2f(lseL - mx2), e1 = __builtin_amdgcn_exp2f(lseR - mx2);
        const float rd2 = __builtin_amdgcn_rcpf(e0 + e1), c0 = e0 * rd2, c1 = e1 * rd2;
#pragma unroll
        for (int j = 0; j < 4; ++j) {
            u32x4 w;
#pragma unroll
            for (int e = 0; e < 4; ++e) {
                const int r = 8 * (j & 1) + 2 * e; const f32x16& oo = o[2 * G + (j >> 1)];
                w[e] = cvtpk(oo[r] * c0 + bflo(f[j][e]) * c1, oo[r + 1] * c0 + bfhi(f[j][e]) * c1);
            }
            lds_w128(xa + (4 * G + j) * 1024, w);
        }
        BARRIER();
    };
    if (g) TAIL(std::integral_constant<int, 1>{}); else TAIL(std::integral_constant<int, 0>{});
    {
        const __amdgpu_buffer_rsrc_t p2 = __builtin_amdgcn_make_buffer_rsrc(ws + WS_P2 + (size_t)(b * 2 + sp) * P2STRIDE, 0, 16 * 36864, 0x00020000);
        auto put = [&](const f32x16& c, unsigned piece) {
#pragma unroll
            for (int u = 0; u < 2; ++u) {
                u32x4 f;
#pragma unroll
                for (int e = 0; e < 4; ++e) f[e] = cvtpk(c[8 * u + 2 * e], c[8 * u + 2 * e + 1]);
                st_sc1(p2, kvh * 36864 + (piece + u) * 1024 + lane * 16, f);
            }
        };
        constexpr int LB = 6;
        const unsigned rbf = wid & 1;
        const unsigned ob[4] = {L0 + xslot(rbf) + lane * 16, L0 + xslot(2 + rbf) + lane * 16, L0 + xslot(rbf ^ 1) + lane * 16, L0 + xslot(2 + (rbf ^ 1)) + lane * 16};
        {
            bf16x8 f[32]; f32x16 c = {}, c2 = {};
            auto rd = [&](int i) { f[i] = lds_b128(ob[i >> 3] + (i & 7) * 1024); };
#pragma unroll
            for (int i = 0; i < LB; ++i) rd(i);
            SBAR();
#pragma unroll
            for (int i = 0; i < 32; ++i) {
                if (i + LB < 32) rd(i + LB);
                if (i < 16) c = MFMA(wf[i], f[i], c); else c2 = MFMA(wf[i - 16], f[i], c2);
                if (i == 19) put(c, wid * 4 + rbf * 2);
                SBAR();
            }
            if (wid < 2) {
                bf16x8 fa[16]; c = f32x16{};
                auto rd8 = [&](int i) { const unsigned a = L0 + KR + 1 * TILEB + wor + i * 1024; fa[i] = cat(lds_tr(a), lds_tr(a + 512)); f[i] = lds_b128(ob[i >> 3] + (i & 7) * 1024); };
#pragma unroll
                for (int i = 0; i < 4; ++i) rd8(i);
                SBAR();
#pragma unroll
                for (int i = 0; i < 16; ++i) {
                    if (i + 4 < 16) rd8(i + 4);
                    c = MFMA(fa[i], f[i], c);
                    if (i == 3) put(c2, wid * 4 + (rbf ^ 1) * 2);
                    SBAR();
                }
                put(c, 32 + wid * 2);
            } else put(c2, wid * 4 + (rbf ^ 1) * 2);
        }
        VMCNT(0);
        GAS u64* f2 = (GAS u64*)(ws + WS_F2);
        {
            const unsigned cons = lane < 2 ? wid * 2 + lane : wid * 8 + (lane - 2);
            if (lane < (wid < 2 ? 10u : 2u)) __hip_atomic_store(f2 + (size_t)((b * HKV + cons) * 2 + sp) * 32 + (lane < 2 ? 0 : 16) + kvh, tag, RLX_AGENT);
        }
        {
            if (wid == 7) wait_flag(f2 + (size_t)me * 32 + (lane & 31), tag);
            const unsigned sg = lane >> 4, c16 = lane & 15;
            const unsigned cc = kvh * 128 + wid * 16 + c16, c8 = 2048 + kvh * 16 + c16;
            auto oaddr = [&](unsigned c) {
                const unsigned piece = c >> 6, ln = c & 63, nb = piece >> 2, rb = (piece >> 1) & 1, u = piece & 1;
                return Out + ((size_t)(b * 64 + rb * 32 + (ln & 31)) * DM + sp * 288 + pcol(nb) * 32 + u * 16 + (ln >> 5) * 4 + (sg >> 1) * 8 + (sg & 1) * 2) * 2;
            };
            char* op = oaddr(cc); char* op8 = oaddr(c8);
            asm volatile("" : "+v"(op), "+v"(op8));
            BAR0();
            auto fin = [&](const u32x4* v, char* o_) {
                float a[8];
#pragma unroll
                for (int e = 0; e < 4; ++e) { a[2 * e] = (bflo(v[0][e]) + bflo(v[1][e])) + (bflo(v[2][e]) + bflo(v[3][e])); a[2 * e + 1] = (bfhi(v[0][e]) + bfhi(v[1][e])) + (bfhi(v[2][e]) + bfhi(v[3][e])); }
                float h[4], q[2];
#pragma unroll
                for (int e = 0; e < 4; ++e) { auto r = __builtin_amdgcn_permlane32_swap(__float_as_uint(a[e]), __float_as_uint(a[e + 4]), false, false); h[e] = __uint_as_float(r[0]) + __uint_as_float(r[1]); }
#pragma unroll
                for (int e = 0; e < 2; ++e) { auto r = __builtin_amdgcn_permlane16_swap(__float_as_uint(h[e]), __float_as_uint(h[e + 2]), false, false); q[e] = __uint_as_float(r[0]) + __uint_as_float(r[1]); }
                *(GAS unsigned*)o_ = cvtpk(q[0], q[1]);
            };
            u32x4 v[4], v8[4];
#pragma unroll
            for (int s = 0; s < 4; ++s) v[s] = ld_sc1(p2, (sg * 4 + s) * 36864 + cc * 16);
            if (wid == 6) {
#pragma unroll
                for (int s = 0; s < 4; ++s) v8[s] = ld_sc1(p2, (sg * 4 + s) * 36864 + c8 * 16);
            }
            if (wid == 0 && kvh == 0 && sp == 0 && lane == 0) __hip_atomic_store((GAS u64*)(ws + WS_EPOCH + b * 256), tag, RLX_AGENT);
            fin(v, op);
            if (wid == 6) fin(v8, op8);
        }
    }
}

extern "C" void kernel_launch(void* const* d_in, const int* in_sizes, int n_in, void* d_out, int out_size, void* d_ws, size_t ws_size, hipStream_t stream) {
    (void)hipFuncSetAttribute((const void*)gqa_fused, hipFuncAttributeMaxDynamicSharedMemorySize, LDS_BYTES);
    hipLaunchKernelGGL(gqa_fused, dim3(256), dim3(512), LDS_BYTES, stream, (const char*)d_in[0], (const char*)d_in[1], (const char*)d_in[2], (const char*)d_in[3],
                       (const char*)d_in[4], (char*)d_out, (char*)d_ws);
}
```
